```python
import torch

def _softmax(x, axis=-1):
    return torch.softmax(x, dim=axis)
def _gelu(x):
    return torch.nn.functional.gelu(x, approximate="tanh")
def _silu(x):
    return torch.nn.functional.silu(x)
def _sigmoid(x):
    return torch.sigmoid(x)
def _layernorm(x, axis=-1, eps=1e-5):
    m = x.mean(dim=axis, keepdim=True)
    v = x.var(dim=axis, keepdim=True, unbiased=False)
    return (x - m) / torch.sqrt(v + eps)
def _scatter_add(tgt, idx, src, axis=0):
    return tgt.clone().index_add_(axis, idx, src)
def _segment_sum(src, seg, n):
    out = torch.zeros((n,) + src.shape[1:], dtype=src.dtype)
    return out.index_add_(0, seg, src)
def _pad(x, pw):
    flat = tuple(v for pair in reversed(pw) for v in pair)
    return torch.nn.functional.pad(x, flat)

kernel_name = 'interleave_layers__gqa_block_cached__sliding_block_cached__decode'

def setup_inputs():

    _w = (lambda a: (lambda t: t.to(torch.bfloat16) if t.is_floating_point() else t)(torch.as_tensor(a)))
    def _A_setup():
        B, Sq, D = 1, 1, 448
        Hq, Hkv, Sk, Dh = 16, 8, 16384, 256
        assert Hq % Hkv == 0, (Hq, Hkv)
        return {
            "x": _w((lambda *s: torch.randn(*s, dtype=torch.bfloat16) * 0.1)(B, Sq, D)),
            "k_cache": _w((lambda *s: torch.randn(*s, dtype=torch.bfloat16) * 0.8)(B, Hkv, Sk, Dh)),
            "v_cache": _w((lambda *s: torch.randn(*s, dtype=torch.bfloat16) * 3.2)(B, Hkv, Sk, Dh)),
            "wq": _w((lambda *s: torch.randn(*s, dtype=torch.bfloat16) * 0.8)(D, Hq * Dh) * (D ** -0.5)),
            "wo": _w((lambda *s: torch.randn(*s, dtype=torch.bfloat16) * 3.2)(Hq * Dh, D) * ((Hq * Dh) ** -0.5)),
        }
    _A_in = _A_setup()

    def _B_setup():
        B, Sq, D = 1, 1, 448
        Hq, Hkv, Sk, Dh = 16, 8, 16384, 256
        assert Hq % Hkv == 0, (Hq, Hkv)
        return {
            "x": _w((lambda *s: torch.randn(*s, dtype=torch.bfloat16) * 0.1)(B, Sq, D)),
            "k_cache": _w((lambda *s: torch.randn(*s, dtype=torch.bfloat16) * 1.6)(B, Hkv, Sk, Dh)),
            "v_cache": _w((lambda *s: torch.randn(*s, dtype=torch.bfloat16) * 1.6)(B, Hkv, Sk, Dh)),
            "wq": _w((lambda *s: torch.randn(*s, dtype=torch.bfloat16) * 1.6)(D, Hq * Dh) * (D ** -0.5)),
            "wo": _w((lambda *s: torch.randn(*s, dtype=torch.bfloat16) * 1.6)(Hq * Dh, D) * ((Hq * Dh) ** -0.5)),
        }
    _B_in = _B_setup()

    return {
        "A_x": _A_in["x"],
        "A_k_cache": _A_in["k_cache"],
        "A_v_cache": _A_in["v_cache"],
        "A_wq": _A_in["wq"],
        "A_wo": _A_in["wo"],
        "B_x": _B_in["x"],
        "B_k_cache": _B_in["k_cache"],
        "B_v_cache": _B_in["v_cache"],
        "B_wq": _B_in["wq"],
        "B_wo": _B_in["wo"],
    }

def reference(A_x, A_k_cache, A_v_cache, A_wq, A_wo, B_x, B_k_cache, B_v_cache, B_wq, B_wo):
    def _A_ref(x, k_cache, v_cache, wq, wo):
        B, Sq, D = x.shape
        Hq, Dh = 16, 256
        Hkv = k_cache.shape[1]
        rep = Hq // Hkv
        q = (lambda x, *d: x.permute(*d))((x @ wq).reshape(B, Sq, Hq, Dh), 0, 2, 1, 3)
        k = (lambda x, n, axis: torch.repeat_interleave(x, n, dim=axis))(k_cache, rep, axis=1)
        v = (lambda x, n, axis: torch.repeat_interleave(x, n, dim=axis))(v_cache, rep, axis=1)
        scores = torch.einsum("bhqd,bhkd->bhqk", q, k) * (Dh ** -0.5)
        p = _softmax(scores, axis=-1)
        o = torch.einsum("bhqk,bhkd->bhqd", p, v)
        return (lambda x, *d: x.permute(*d))(o, 0, 2, 1, 3).reshape(B, Sq, Hq * Dh) @ wo

    def _B_ref(x, k_cache, v_cache, wq, wo):
        B, Sq, D = x.shape
        Hq, Dh = 16, 256
        Hkv, Sk = k_cache.shape[1], k_cache.shape[2]
        W = min(4096, max(256, Sk // 4))
        rep = Hq // Hkv
        q = (lambda x, *d: x.permute(*d))((x @ wq).reshape(B, Sq, Hq, Dh), 0, 2, 1, 3)
        k = (lambda x, n, axis: torch.repeat_interleave(x, n, dim=axis))(k_cache[:, :, Sk - W:, :], rep, axis=1)
        v = (lambda x, n, axis: torch.repeat_interleave(x, n, dim=axis))(v_cache[:, :, Sk - W:, :], rep, axis=1)
        scores = torch.einsum("bhqd,bhkd->bhqk", q, k) * (Dh ** -0.5)
        p = _softmax(scores, axis=-1)
        o = torch.einsum("bhqk,bhkd->bhqd", p, v)
        return (lambda x, *d: x.permute(*d))(o, 0, 2, 1, 3).reshape(B, Sq, Hq * Dh) @ wo

    x = A_x
    x = x + _A_ref(_layernorm(x, axis=-1), A_k_cache, A_v_cache, A_wq, A_wo)
    x = x + _B_ref(_layernorm(x, axis=-1), B_k_cache, B_v_cache, B_wq, B_wo)
    x = x + _A_ref(_layernorm(x, axis=-1), A_k_cache, A_v_cache, A_wq, A_wo)
    x = x + _B_ref(_layernorm(x, axis=-1), B_k_cache, B_v_cache, B_wq, B_wo)
    return x
```

```cpp
#include <hip/hip_runtime.h>
#include <hip/hip_bf16.h>
#include <stdint.h>

typedef unsigned short u16;
typedef unsigned u32x4 __attribute__((ext_vector_type(4)));
typedef float f32x4 __attribute__((ext_vector_type(4)));
typedef __bf16 bf16x8 __attribute__((ext_vector_type(8)));
typedef __bf16 bf16x2 __attribute__((ext_vector_type(2)));
typedef float f32x2 __attribute__((ext_vector_type(2)));

constexpr int D = 448, HQ = 16, HKV = 8, SK = 16384, DH = 256, WIN = 4096, QD = HQ * DH;

__device__ __forceinline__ float bf2f(unsigned b) { return __builtin_bit_cast(float, b << 16); }
__device__ __forceinline__ unsigned cvtpk(float lo, float hi) {
  f32x2 v = {lo, hi}; bf16x2 b = __builtin_convertvector(v, bf16x2); return __builtin_bit_cast(unsigned, b);
}
__device__ __forceinline__ f32x4 mfma(u32x4 a, u32x4 b, f32x4 c) {
  return __builtin_amdgcn_mfma_f32_16x16x32_bf16(__builtin_bit_cast(bf16x8, a), __builtin_bit_cast(bf16x8, b), c, 0, 0, 0);
}

__global__ void __launch_bounds__(256) k_qproj(const u16* __restrict__ x, const u16* __restrict__ wq, u16* __restrict__ q) {
  __shared__ float xs[D];
  __shared__ float red[256 * 8];
  __shared__ float st[8];
  const int t = threadIdx.x, b = blockIdx.x;
  float s = 0.f;
  for (int i = t; i < D; i += 256) { float v = bf2f(x[i]); xs[i] = v; s += v; }
  for (int o = 32; o; o >>= 1) s += __shfl_xor(s, o);
  if ((t & 63) == 0) st[t >> 6] = s;
  __syncthreads();
  const float mean = (st[0] + st[1] + st[2] + st[3]) * (1.f / D);
  float s2 = 0.f;
  for (int i = t; i < D; i += 256) { float v = xs[i] - mean; s2 += v * v; }
  for (int o = 32; o; o >>= 1) s2 += __shfl_xor(s2, o);
  if ((t & 63) == 0) st[4 + (t >> 6)] = s2;
  __syncthreads();
  const float rstd = rsqrtf((st[4] + st[5] + st[6] + st[7]) * (1.f / D) + 1e-5f);
  float acc[8] = {0, 0, 0, 0, 0, 0, 0, 0};
  const int half = t & 1;
  for (int k = t >> 1; k < D; k += 128) {
    u32x4 w = *(const u32x4*)(wq + (size_t)k * QD + 16 * b + 8 * half);
    float xv = (xs[k] - mean) * rstd;
#pragma unroll
    for (int i = 0; i < 4; ++i) { acc[2 * i] += xv * bf2f(w[i] & 0xffff); acc[2 * i + 1] += xv * bf2f(w[i] >> 16); }
  }
#pragma unroll
  for (int i = 0; i < 8; ++i) red[t * 8 + i] = acc[i];
  __syncthreads();
  if (t < 16) {
    float r = 0.f;
    for (int k = 0; k < 128; ++k) r += red[(2 * k + (t >> 3)) * 8 + (t & 7)];
    q[16 * b + t] = (u16)(cvtpk(r, 0.f) & 0xffff);
  }
}

template <int NG>
__global__ void __launch_bounds__(256) k_attn(const u16* __restrict__ q, const u16* __restrict__ K, const u16* __restrict__ V,
                                              int key0, float* __restrict__ part  ) {
  __shared__ float mrg[4][2 * 256 + 4];
  const int t = threadIdx.x, b = blockIdx.x, lane = t & 63, w = t >> 6, g = b & 7, j = b >> 3;
  const int c = lane & 15, gg = lane >> 4;
  const int kbase = key0 + (j * 4 + w) * (NG * 32);
  const u16* Kp = K + ((size_t)g * SK + kbase) * DH;
  const u16* Vp = V + ((size_t)g * SK + kbase) * DH;
  u32x4 qf[8];
#pragma unroll
  for (int ch = 0; ch < 8; ++ch) qf[ch] = *(const u32x4*)(q + (2 * g + (c & 1)) * DH + 32 * ch + 8 * gg);
  f32x4 S[2 * NG];
#pragma unroll
  for (int tl = 0; tl < 2 * NG; ++tl) {
    f32x4 a = {0, 0, 0, 0};
#pragma unroll
    for (int ch = 0; ch < 8; ++ch) {
      u32x4 kf = __builtin_nontemporal_load((const u32x4*)(Kp + (size_t)(16 * tl + c) * DH + 32 * ch + 8 * gg));
      a = mfma(kf, qf[ch], a);
    }
    S[tl] = a;
  }
  constexpr float C2 = 0.0625f * 1.4426950408889634f;
  float m = -INFINITY;
#pragma unroll
  for (int tl = 0; tl < 2 * NG; ++tl)
#pragma unroll
    for (int r = 0; r < 4; ++r) m = fmaxf(m, S[tl][r]);
  m = fmaxf(m, __shfl_xor(m, 16)); m = fmaxf(m, __shfl_xor(m, 32));
  const float mc = m * C2;
  float l = 0.f;
  u32x4 P[NG];
#pragma unroll
  for (int kg = 0; kg < NG; ++kg) {
    float p[8];
#pragma unroll
    for (int r = 0; r < 4; ++r) { p[r] = __builtin_amdgcn_exp2f(S[2 * kg][r] * C2 - mc); p[4 + r] = __builtin_amdgcn_exp2f(S[2 * kg + 1][r] * C2 - mc); }
#pragma unroll
    for (int i = 0; i < 4; ++i) { unsigned pk = cvtpk(p[2 * i], p[2 * i + 1]); P[kg][i] = pk; l += bf2f(pk & 0xffff) + bf2f(pk >> 16); }
  }
  l += __shfl_xor(l, 16); l += __shfl_xor(l, 32);
#pragma unroll
  for (int half = 0; half < 2; ++half) {
    f32x4 O[8];
#pragma unroll
    for (int d = 0; d < 8; ++d) O[d] = f32x4{0, 0, 0, 0};
#pragma unroll
    for (int kg = 0; kg < NG; ++kg) {
      u32x4 vr[8];
#pragma unroll
      for (int jj = 0; jj < 8; ++jj) {
        int key = 32 * kg + 16 * (jj >> 2) + 4 * gg + (jj & 3);
        vr[jj] = __builtin_nontemporal_load((const u32x4*)(Vp + (size_t)key * DH + 128 * half + 8 * c));
      }
#pragma unroll
      for (int d = 0; d < 8; ++d) {
        u32x4 vf;
#pragma unroll
        for (int jj = 0; jj < 4; ++jj) {
          unsigned lo = vr[2 * jj][d >> 1], hi = vr[2 * jj + 1][d >> 1];
          vf[jj] = (d & 1) ? __builtin_amdgcn_perm(hi, lo, 0x07060302) : __builtin_amdgcn_perm(hi, lo, 0x05040100);
        }
        O[d] = mfma(P[kg], vf, O[d]);
      }
    }
    if (gg < 2) {
#pragma unroll
      for (int d = 0; d < 8; ++d) mrg[w][gg * 256 + 128 * half + 8 * c + d] = gg ? O[d][1] : O[d][0];
    }
  }
  if (lane < 2) { mrg[w][512 + lane] = mc; mrg[w][514 + lane] = l; }
  __syncthreads();
  for (int i = t; i < 512; i += 256) {
    const int h = i >> 8;
    float M = fmaxf(fmaxf(mrg[0][512 + h], mrg[1][512 + h]), fmaxf(mrg[2][512 + h], mrg[3][512 + h]));
    float o = 0.f, L = 0.f;
#pragma unroll
    for (int ww = 0; ww < 4; ++ww) { float wt = __builtin_amdgcn_exp2f(mrg[ww][512 + h] - M); o += wt * mrg[ww][i]; L += wt * mrg[ww][514 + h]; }
    part[(size_t)b * 516 + i] = o;
    if ((i & 255) == 0) { part[(size_t)b * 516 + 512 + h] = M; part[(size_t)b * 516 + 514 + h] = L; }
  }
}

__global__ void __launch_bounds__(256) k_comb(const float* __restrict__ part, const u16* __restrict__ wo, float* __restrict__ px) {
  __shared__ float o[16];
  const int t = threadIdx.x, b = blockIdx.x, g = b & 7, j = b >> 3, h = j >> 4, d0 = 16 * (j & 15);
  if (t < 16) {
    float M = -INFINITY;
    for (int i = 0; i < 32; ++i) M = fmaxf(M, part[(size_t)(i * 8 + g) * 516 + 512 + h]);
    float L = 0.f, a = 0.f;
    for (int i = 0; i < 32; ++i) {
      const float* p = part + (size_t)(i * 8 + g) * 516;
      float wt = __builtin_amdgcn_exp2f(p[512 + h] - M);
      L += wt * p[514 + h]; a += wt * p[h * 256 + d0 + t];
    }
    o[t] = bf2f(cvtpk(a / L, 0.f) & 0xffff);
  }
  __syncthreads();
  for (int col = t; col < D; col += 256) {
    float s = 0.f;
    for (int r = 0; r < 16; ++r) s += o[r] * bf2f(wo[(size_t)((2 * g + h) * 256 + d0 + r) * D + col]);
    px[(size_t)b * D + col] = s;
  }
}

__global__ void __launch_bounds__(256) k_red(const float* __restrict__ px, const u16* __restrict__ xin, u16* __restrict__ xout) {
  __shared__ float red[256];
  const int t = threadIdx.x, col = blockIdx.x * 64 + (t & 63), rg = t >> 6;
  float s = 0.f;
  for (int i = rg; i < 256; i += 4) s += px[(size_t)i * D + col];
  red[t] = s;
  __syncthreads();
  if (t < 64) {
    float a = red[t] + red[t + 64] + red[t + 128] + red[t + 192];
    a = bf2f(cvtpk(a, 0.f) & 0xffff);
    xout[col] = (u16)(cvtpk(bf2f(xin[col]) + a, 0.f) & 0xffff);
  }
}

extern "C" void kernel_launch(void* const* d_in, const int* in_sizes, int n_in, void* d_out, int out_size, void* d_ws, size_t ws_size,
                              hipStream_t stream) {
  const u16* Ax = (const u16*)d_in[0];
  const u16* Kc[2] = {(const u16*)d_in[1], (const u16*)d_in[6]};
  const u16* Vc[2] = {(const u16*)d_in[2], (const u16*)d_in[7]};
  const u16* Wq[2] = {(const u16*)d_in[3], (const u16*)d_in[8]};
  const u16* Wo[2] = {(const u16*)d_in[4], (const u16*)d_in[9]};
  char* ws = (char*)d_ws;
  u16* q = (u16*)ws;
  u16* xb[2] = {(u16*)(ws + 16384), (u16*)(ws + 32768)};
  float* part = (float*)(ws + 65536);
  float* px = (float*)(ws + 65536 + 1024 * 1024);
  const u16* xin = Ax;
  for (int l = 0; l < 4; ++l) {
    const int s = l & 1;
    u16* xout = (l == 3) ? (u16*)d_out : xb[l & 1];
    k_qproj<<<256, 256, 0, stream>>>(xin, Wq[s], q);
    if (s == 0) k_attn<4><<<256, 256, 0, stream>>>(q, Kc[s], Vc[s], 0, part);
    else k_attn<1><<<256, 256, 0, stream>>>(q, Kc[s], Vc[s], SK - WIN, part);
    k_comb<<<256, 256, 0, stream>>>(part, Wo[s], px);
    k_red<<<7, 256, 0, stream>>>(px, xin, xout);
    xin = xout;
  }
}
```

```cpp
#include <hip/hip_runtime.h>
#include <hip/hip_bf16.h>
#include <stdint.h>

struct Params {
  const void* kB; const void* vA; const void* vB; const void* wqA; const void* wqB; const void* woA; const void* woB; void* out;
  const void* kA; const void* x; void* ws;
  int tune[8];
};
static const int g_tune[8] = {12,12,12,135,100,490,520,370};

__global__ void __launch_bounds__(256, 1) mega_decode(Params p) {
  __shared__ __attribute__((aligned(16))) char lds[163840];
  unsigned tid = threadIdx.x;
  asm volatile(
    "L_CODE0:\n"
    "s_getpc_b64 s[20:21]\n"
    "s_load_dwordx16 s[28:43], s[0:1], 0\n"
    "s_load_dwordx2 s[8:9], s[0:1], 64\n"
    "s_load_dwordx2 s[24:25], s[0:1], 72\n"
    "s_load_dwordx2 s[4:5], s[0:1], 80\n"
    "s_load_dwordx8 s[80:87], s[0:1], 88\n"
    "s_and_b32 s21, s21, 0xffff\n"
    "s_mov_b32 s22, L_CODE1-L_CODE0\n"
    "s_mov_b32 s23, 0x20000\n"
    "s_mov_b32 m0, 10240\n"
    "v_lshlrev_b32 v1, 6, v0\n"
    "s_movk_i32 s54, 16384\n"
    "buffer_load_dword v1, s[20:23], 0 offen lds\n"
    "buffer_load_dword v1, s[20:23], s54 offen lds\n"
    "v_lshrrev_b32 v1, 6, v0\n"
    "v_and_b32 v0, 63, v0\n"
    "v_readfirstlane_b32 s44, v1\n"
    "s_and_b32 s45, s2, 7\n"
    "s_lshr_b32 s46, s2, 3\n"
    "v_bfe_u32 v8, v0, 1, 2\n"
    "v_bfe_u32 v9, v0, 4, 2\n"
    "v_xor_b32 v8, v8, v9\n"
    "v_lshlrev_b32 v8, 5, v8\n"
    "v_and_b32 v9, 1, v0\n"
    "v_lshl_or_b32 v8, v9, 4, v8\n"
    "v_lshrrev_b32 v9, 3, v0\n"
    "v_lshl_or_b32 v1, v9, 9, v8\n"
    "s_lshl_b32 s54, s45, 23\n"
    "s_lshl_b32 s55, s46, 18\n"
    "s_add_u32 s55, s55, s54\n"
    "s_and_b32 s58, s46, 15\n"
    "s_lshl_b32 s58, s58, 14\n"
    "s_lshl_b32 s56, s44, 16\n"
    "s_add_u32 s49, s58, s56\n"
    "s_lshl_b32 s57, s44, 7\n"
    "s_add_u32 s50, s55, s57\n"
    "s_mul_i32 s52, s44, 36864\n"
    "s_add_u32 s52, s52, 16384\n"
    "s_mov_b32 s14, 67108864\n"
    "s_mov_b32 s15, 0x20000\n"
    "s_mov_b32 s10, 67108864\n"
    "s_mov_b32 s11, 0x20000\n"
    "s_mov_b32 s6, 5242880\n"
    "s_mov_b32 s7, 0x20000\n"
    "s_mov_b32 s18, 3670016\n"
    "s_mov_b32 s19, 0x20000\n"
    "s_mov_b32 s22, 3670016\n"
    "s_mov_b32 s23, 0x20000\n"
    "s_mov_b32 s26, 896\n"
    "s_mov_b32 s27, 0x20000\n"
    "s_mov_b32 s47, 0\n"
    "s_mov_b32 s79, 0\n"
    "s_waitcnt lgkmcnt(0)\n"
    "s_and_b32 s31, s31, 0xffff\n"
    "s_and_b32 s9, s9, 0xffff\n"
    "s_and_b32 s25, s25, 0xffff\n"
    "s_and_b32 s35, s35, 0xffff\n"
    "s_and_b32 s39, s39, 0xffff\n"
    "s_and_b32 s29, s29, 0xffff\n"
    "s_and_b32 s33, s33, 0xffff\n"
    "s_and_b32 s37, s37, 0xffff\n"
    "s_and_b32 s41, s41, 0xffff\n"
    "s_and_b32 s43, s43, 0xffff\n"
    "s_and_b32 s5, s5, 0xffff\n"
    "s_mov_b64 s[12:13], s[30:31]\n"
    "s_mov_b32 s1, s58\n"
    "s_lshl_b32 s55, s46, 16\n"
    "s_add_u32 s55, s55, s54\n"
    "s_add_u32 s55, s55, 6291456\n"
    "s_add_u32 s51, s55, s57\n"
    "s_lshl_b32 s56, s44, 14\n"
    "s_add_u32 s78, s55, s56\n"
    "s_mov_b32 s53, 327680\n"
    "s_mov_b32 s48, 0\n"
    "s_mov_b32 s73, 131072\n"
    "s_mov_b32 s66, 0x07060302\n"
    "s_mov_b32 s74, 0x000f000f\n"
    "s_mov_b32 s75, 0x000f000f\n"
    "s_mov_b32 s76, 0x33333333\n"
    "s_mov_b32 s77, 0x33333333\n"
    "s_lshr_b32 s58, s46, 3\n"
    "s_and_b32 s59, s46, 7\n"
    "s_lshl_b32 s54, s45, 9\n"
    "s_lshl_b32 s55, s58, 7\n"
    "s_add_u32 s54, s54, s55\n"
    "s_lshl_b32 s55, s44, 5\n"
    "s_add_u32 s54, s54, s55\n"
    "s_mul_i32 s54, s54, 896\n"
    "s_mul_i32 s55, s59, 112\n"
    "s_add_u32 s71, s54, s55\n"
    "s_mul_i32 s60, s58, 112\n"
    "s_mul_i32 s55, s44, 28\n"
    "s_add_u32 s60, s60, s55\n"
    "s_lshl_b32 s54, s60, 13\n"
    "s_lshl_b32 s55, s45, 10\n"
    "s_add_u32 s54, s54, s55\n"
    "s_lshl_b32 s55, s59, 7\n"
    "s_add_u32 s72, s54, s55\n"
    "s_lshl_b32 s60, s60, 1\n"
    "v_lshrrev_b32 v11, 5, v0\n"
    "v_lshlrev_b32 v11, 2, v11\n"
    "v_and_or_b32 v10, v0, 3, v11\n"
    "v_bfe_u32 v11, v0, 2, 3\n"
    "v_lshrrev_b32 v12, 3, v0\n"
    "v_lshlrev_b32 v12, 15, v12\n"
    "v_and_b32 v14, 7, v0\n"
    "v_lshl_or_b32 v12, v14, 4, v12\n"
    "v_cmp_lt_u32 vcc, 55, v0\n"
    "v_mov_b32 v14, 1073741824\n"
    "s_nop 0\n"
    "v_cndmask_b32 v12, v12, v14, vcc\n"
    "v_cmp_eq_u32 vcc, 7, v10\n"
    "v_lshlrev_b32 v13, 3, v10\n"
    "s_nop 0\n"
    "v_cndmask_b32 v13, v13, v14, vcc\n"
    "v_mov_b32 v15, 0\n"
    "s_mov_b64 s[16:17], s[34:35]\n"
    "buffer_load_dwordx2 v[24:25], v15, s[4:7], 0 offen offset:0 sc1\n"
    "buffer_load_dwordx2 v[26:27], v13, s[24:27], s60 offen\n"
    "s_add_u32 s63, s72, 0\n"
    "buffer_load_dwordx4 v[68:71], v12, s[16:19], s63 offen nt\n"
    "s_add_u32 s63, s72, 8192\n"
    "buffer_load_dwordx4 v[76:79], v12, s[16:19], s63 offen nt\n"
    "s_add_u32 s63, s72, 16384\n"
    "buffer_load_dwordx4 v[84:87], v12, s[16:19], s63 offen nt\n"
    "s_add_u32 s63, s72, 24576\n"
    "buffer_load_dwordx4 v[92:95], v12, s[16:19], s63 offen nt\n"
    "s_mov_b64 s[16:17], s[36:37]\n"
    "s_add_u32 s63, s72, 0\n"
    "buffer_load_dwordx4 v[100:103], v12, s[16:19], s63 offen nt\n"
    "s_add_u32 s63, s72, 8192\n"
    "buffer_load_dwordx4 v[108:111], v12, s[16:19], s63 offen nt\n"
    "s_add_u32 s63, s72, 16384\n"
    "buffer_load_dwordx4 v[116:119], v12, s[16:19], s63 offen nt\n"
    "s_add_u32 s63, s72, 24576\n"
    "buffer_load_dwordx4 v[124:127], v12, s[16:19], s63 offen nt\n"
    "v_mov_b32 v28, -1\n"
    "v_mov_b32 v29, -1\n"
    "v_mov_b32 v30, -1\n"
    "v_mov_b32 v31, -1\n"
    "v_lshlrev_b32 v32, 4, v0\n"
    "s_lshl_b32 s54, s2, 2\n"
    "s_add_u32 s54, s54, s44\n"
    "s_mul_i32 s54, s54, 2816\n"
    "s_add_u32 s54, s54, 327680\n"
    "buffer_store_dwordx4 v[28:31], v32, s[4:7], s54 offen sc1\n"
    "buffer_store_dwordx4 v[28:31], v32, s[4:7], s54 offen offset:1024 sc1\n"
    "s_bfm_b64 exec, 48, 0\n"
    "buffer_store_dwordx4 v[28:31], v32, s[4:7], s54 offen offset:2048 sc1\n"
    "s_mov_b64 exec, -1\n"
    "s_add_u32 s61, s1, 212992\n"
    "s_and_b32 s61, s61, 0x3ffff\n"
    "s_add_u32 s61, s61, s50\n"
    "s_add_u32 m0, s52, 16384\n"
    "s_add_u32 s63, s61, 0\n"
    "buffer_load_dwordx4 v1, s[12:15], s63 offen offset:0 nt lds\n"
    "s_add_u32 s63, s61, 3072\n"
    "buffer_load_dwordx4 v1, s[12:15], s63 offen offset:1024 nt lds\n"
    "s_add_u32 s63, s61, 6144\n"
    "buffer_load_dwordx4 v1, s[12:15], s63 offen offset:2048 nt lds\n"
    "s_add_u32 s63, s61, 9216\n"
    "buffer_load_dwordx4 v1, s[12:15], s63 offen offset:3072 nt lds\n"
    "s_add_u32 s61, s1, 212992\n"
    "s_and_b32 s61, s61, 0x3ffff\n"
    "s_add_u32 s61, s61, s50\n"
    "s_add_u32 m0, s52, 16384\n"
    "s_add_u32 s63, s61, 0\n"
    "buffer_load_dwordx4 v1, s[12:15], s63 offen offset:0 sc1 lds\n"
    "s_add_u32 s63, s61, 3072\n"
    "buffer_load_dwordx4 v1, s[12:15], s63 offen offset:1024 sc1 lds\n"
    "s_add_u32 s63, s61, 6144\n"
    "buffer_load_dwordx4 v1, s[12:15], s63 offen offset:2048 sc1 lds\n"
    "s_add_u32 s63, s61, 9216\n"
    "buffer_load_dwordx4 v1, s[12:15], s63 offen offset:3072 sc1 lds\n"
    "s_add_u32 s61, s1, 229376\n"
    "s_and_b32 s61, s61, 0x3ffff\n"
    "s_add_u32 s61, s61, s50\n"
    "s_add_u32 m0, s52, 20480\n"
    "s_add_u32 s63, s61, 0\n"
    "buffer_load_dwordx4 v1, s[12:15], s63 offen offset:0 nt lds\n"
    "s_add_u32 s63, s61, 3072\n"
    "buffer_load_dwordx4 v1, s[12:15], s63 offen offset:1024 nt lds\n"
    "s_add_u32 s63, s61, 6144\n"
    "buffer_load_dwordx4 v1, s[12:15], s63 offen offset:2048 nt lds\n"
    "s_add_u32 s63, s61, 9216\n"
    "buffer_load_dwordx4 v1, s[12:15], s63 offen offset:3072 nt lds\n"
    "s_add_u32 s61, s1, 229376\n"
    "s_and_b32 s61, s61, 0x3ffff\n"
    "s_add_u32 s61, s61, s50\n"
    "s_add_u32 m0, s52, 20480\n"
    "s_add_u32 s63, s61, 0\n"
    "buffer_load_dwordx4 v1, s[12:15], s63 offen offset:0 sc1 lds\n"
    "s_add_u32 s63, s61, 3072\n"
    "buffer_load_dwordx4 v1, s[12:15], s63 offen offset:1024 sc1 lds\n"
    "s_add_u32 s63, s61, 6144\n"
    "buffer_load_dwordx4 v1, s[12:15], s63 offen offset:2048 sc1 lds\n"
    "s_add_u32 s63, s61, 9216\n"
    "buffer_load_dwordx4 v1, s[12:15], s63 offen offset:3072 sc1 lds\n"
    "s_add_u32 s61, s1, 245760\n"
    "s_and_b32 s61, s61, 0x3ffff\n"
    "s_add_u32 s61, s61, s50\n"
    "s_add_u32 m0, s52, 24576\n"
    "s_add_u32 s63, s61, 0\n"
    "buffer_load_dwordx4 v1, s[12:15], s63 offen offset:0 nt lds\n"
    "s_add_u32 s63, s61, 3072\n"
    "buffer_load_dwordx4 v1, s[12:15], s63 offen offset:1024 nt lds\n"
    "s_add_u32 s63, s61, 6144\n"
    "buffer_load_dwordx4 v1, s[12:15], s63 offen offset:2048 nt lds\n"
    "s_add_u32 s63, s61, 9216\n"
    "buffer_load_dwordx4 v1, s[12:15], s63 offen offset:3072 nt lds\n"
    "s_add_u32 s61, s1, 245760\n"
    "s_and_b32 s61, s61, 0x3ffff\n"
    "s_add_u32 s61, s61, s50\n"
    "s_add_u32 m0, s52, 24576\n"
    "s_add_u32 s63, s61, 0\n"
    "buffer_load_dwordx4 v1, s[12:15], s63 offen offset:0 sc1 lds\n"
    "s_add_u32 s63, s61, 3072\n"
    "buffer_load_dwordx4 v1, s[12:15], s63 offen offset:1024 sc1 lds\n"
    "s_add_u32 s63, s61, 6144\n"
    "buffer_load_dwordx4 v1, s[12:15], s63 offen offset:2048 sc1 lds\n"
    "s_add_u32 s63, s61, 9216\n"
    "buffer_load_dwordx4 v1, s[12:15], s63 offen offset:3072 sc1 lds\n"
    "v_lshrrev_b32 v8, 5, v0\n"
    "v_and_b32 v9, 31, v0\n"
    "v_xor_b32 v9, v9, v8\n"
    "v_lshlrev_b32 v9, 4, v9\n"
    "v_lshl_or_b32 v128, v8, 9, v9\n"
    "v_xor_b32 v129, 32, v128\n"
    "v_xor_b32 v130, 64, v128\n"
    "v_xor_b32 v131, 96, v128\n"
    "v_xor_b32 v132, 128, v128\n"
    "v_xor_b32 v133, 160, v128\n"
    "v_xor_b32 v134, 192, v128\n"
    "v_xor_b32 v135, 224, v128\n"
    "s_add_u32 s61, s49, 0\n"
    "s_and_b32 s61, s61, 0x3ffff\n"
    "s_andn2_b32 s63, s50, 0x1ff\n"
    "s_add_u32 s61, s61, s63\n"
    "s_add_u32 m0, s52, 0\n"
    "s_add_u32 s63, s61, 0\n"
    "buffer_load_dwordx4 v128, s[8:11], s63 offen offset:0 nt lds\n"
    "buffer_load_dwordx4 v129, s[8:11], s63 offen offset:1024 nt lds\n"
    "buffer_load_dwordx4 v130, s[8:11], s63 offen offset:2048 nt lds\n"
    "buffer_load_dwordx4 v131, s[8:11], s63 offen offset:3072 nt lds\n"
    "s_add_u32 m0, s52, 4096\n"
    "s_add_u32 s63, s61, 4096\n"
    "buffer_load_dwordx4 v132, s[8:11], s63 offen offset:0 nt lds\n"
    "buffer_load_dwordx4 v133, s[8:11], s63 offen offset:1024 nt lds\n"
    "buffer_load_dwordx4 v134, s[8:11], s63 offen offset:2048 nt lds\n"
    "buffer_load_dwordx4 v135, s[8:11], s63 offen offset:3072 nt lds\n"
    "v_mov_b32 v36, 0\n"
    "v_mov_b32 v37, 0\n"
    "v_mov_b32 v38, 0\n"
    "v_mov_b32 v39, 0\n"
    "ds_write_b128 v32, v[36:39] offset:0\n"
    "ds_write_b128 v32, v[36:39] offset:4480\n"
    "ds_write_b128 v32, v[36:39] offset:4608\n"
    "s_waitcnt vmcnt(39)\n"
    "v_readfirstlane_b32 s64, v24\n"
    "v_readfirstlane_b32 s65, v25\n"
    "s_add_u32 s64, s64, 1\n"
    "s_addc_u32 s65, s65, 0\n"
    "v_lshlrev_b32 v136, 16, v26\n"
    "v_and_b32 v137, 0xffff0000, v26\n"
    "v_lshlrev_b32 v138, 16, v27\n"
    "v_and_b32 v139, 0xffff0000, v27\n"
    "v_cmp_eq_u32 vcc, 7, v10\n"
    "s_nop 1\n"
    "v_cndmask_b32 v140, 1.0, v15, vcc\n"
    "v_lshrrev_b32 v9, 5, v0\n"
    "v_lshlrev_b32 v9, 2, v9\n"
    "v_and_or_b32 v8, v0, 3, v9\n"
    "v_bfe_u32 v9, v0, 2, 3\n"
    "v_lshlrev_b32 v9, 2, v9\n"
    "v_lshl_or_b32 v8, v8, 5, v9\n"
    "ds_bpermute_b32 v12, v8, v68\n"
    "ds_bpermute_b32 v13, v8, v69\n"
    "ds_bpermute_b32 v14, v8, v70\n"
    "ds_bpermute_b32 v15, v8, v71\n"
    "ds_bpermute_b32 v20, v8, v76\n"
    "ds_bpermute_b32 v21, v8, v77\n"
    "ds_bpermute_b32 v22, v8, v78\n"
    "ds_bpermute_b32 v23, v8, v79\n"
    "ds_bpermute_b32 v28, v8, v84\n"
    "ds_bpermute_b32 v29, v8, v85\n"
    "ds_bpermute_b32 v30, v8, v86\n"
    "ds_bpermute_b32 v31, v8, v87\n"
    "ds_bpermute_b32 v36, v8, v92\n"
    "ds_bpermute_b32 v37, v8, v93\n"
    "ds_bpermute_b32 v38, v8, v94\n"
    "ds_bpermute_b32 v39, v8, v95\n"
    "s_waitcnt lgkmcnt(0)\n"
    "v_lshlrev_b32 v8, 16, v12\n"
    "v_and_b32 v9, 0xffff0000, v12\n"
    "v_lshlrev_b32 v10, 16, v13\n"
    "v_and_b32 v11, 0xffff0000, v13\n"
    "v_lshlrev_b32 v12, 16, v14\n"
    "v_and_b32 v13, 0xffff0000, v14\n"
    "v_lshlrev_b32 v14, 16, v15\n"
    "v_and_b32 v15, 0xffff0000, v15\n"
    "v_lshlrev_b32 v16, 16, v20\n"
    "v_and_b32 v17, 0xffff0000, v20\n"
    "v_lshlrev_b32 v18, 16, v21\n"
    "v_and_b32 v19, 0xffff0000, v21\n"
    "v_lshlrev_b32 v20, 16, v22\n"
    "v_and_b32 v21, 0xffff0000, v22\n"
    "v_lshlrev_b32 v22, 16, v23\n"
    "v_and_b32 v23, 0xffff0000, v23\n"
    "v_lshlrev_b32 v24, 16, v28\n"
    "v_and_b32 v25, 0xffff0000, v28\n"
    "v_lshlrev_b32 v26, 16, v29\n"
    "v_and_b32 v27, 0xffff0000, v29\n"
    "v_lshlrev_b32 v28, 16, v30\n"
    "v_and_b32 v29, 0xffff0000, v30\n"
    "v_lshlrev_b32 v30, 16, v31\n"
    "v_and_b32 v31, 0xffff0000, v31\n"
    "v_lshlrev_b32 v32, 16, v36\n"
    "v_and_b32 v33, 0xffff0000, v36\n"
    "v_lshlrev_b32 v34, 16, v37\n"
    "v_and_b32 v35, 0xffff0000, v37\n"
    "v_lshlrev_b32 v36, 16, v38\n"
    "v_and_b32 v37, 0xffff0000, v38\n"
    "v_lshlrev_b32 v38, 16, v39\n"
    "v_and_b32 v39, 0xffff0000, v39\n"
    "v_mov_b32 v4, v140\n"
    "v_mov_b32 v5, v140\n"
    "v_mov_b32 v6, v140\n"
    "v_mov_b32 v7, v140\n"
    "s_waitcnt lgkmcnt(0)\n"
    "s_barrier\n"
    "s_mov_b32 s68, 65536\n"
    "s_mov_b32 s48, 1\n"
    "s_nop 4\n"
    "s_branch L_WQ_GEMV\n"
    "L_RET1:\n"
    "s_add_u32 s61, s49, 8192\n"
    "s_and_b32 s61, s61, 0x3ffff\n"
    "s_andn2_b32 s63, s50, 0x1ff\n"
    "s_add_u32 s61, s61, s63\n"
    "s_add_u32 m0, s52, 8192\n"
    "s_add_u32 s63, s61, 0\n"
    "buffer_load_dwordx4 v128, s[8:11], s63 offen offset:0 nt lds\n"
    "buffer_load_dwordx4 v129, s[8:11], s63 offen offset:1024 nt lds\n"
    "buffer_load_dwordx4 v130, s[8:11], s63 offen offset:2048 nt lds\n"
    "buffer_load_dwordx4 v131, s[8:11], s63 offen offset:3072 nt lds\n"
    "s_add_u32 m0, s52, 12288\n"
    "s_add_u32 s63, s61, 4096\n"
    "buffer_load_dwordx4 v132, s[8:11], s63 offen offset:0 nt lds\n"
    "buffer_load_dwordx4 v133, s[8:11], s63 offen offset:1024 nt lds\n"
    "buffer_load_dwordx4 v134, s[8:11], s63 offen offset:2048 nt lds\n"
    "buffer_load_dwordx4 v135, s[8:11], s63 offen offset:3072 nt lds\n"
    "v_mov_b32 v4, v136\n"
    "v_mov_b32 v5, v137\n"
    "v_mov_b32 v6, v138\n"
    "v_mov_b32 v7, v139\n"
    "s_barrier\n"
    "s_mov_b32 s68, 204800\n"
    "s_mov_b32 s48, 2\n"
    "s_nop 4\n"
    "s_branch L_WQ_GEMV\n"
    "L_RET2:\n"
    "s_waitcnt vmcnt(40)\n"
    "v_lshrrev_b32 v9, 5, v0\n"
    "v_lshlrev_b32 v9, 2, v9\n"
    "v_and_or_b32 v8, v0, 3, v9\n"
    "v_bfe_u32 v9, v0, 2, 3\n"
    "v_lshlrev_b32 v9, 2, v9\n"
    "v_lshl_or_b32 v8, v8, 5, v9\n"
    "ds_bpermute_b32 v12, v8, v100\n"
    "ds_bpermute_b32 v13, v8, v101\n"
    "ds_bpermute_b32 v14, v8, v102\n"
    "ds_bpermute_b32 v15, v8, v103\n"
    "ds_bpermute_b32 v20, v8, v108\n"
    "ds_bpermute_b32 v21, v8, v109\n"
    "ds_bpermute_b32 v22, v8, v110\n"
    "ds_bpermute_b32 v23, v8, v111\n"
    "ds_bpermute_b32 v28, v8, v116\n"
    "ds_bpermute_b32 v29, v8, v117\n"
    "ds_bpermute_b32 v30, v8, v118\n"
    "ds_bpermute_b32 v31, v8, v119\n"
    "ds_bpermute_b32 v36, v8, v124\n"
    "ds_bpermute_b32 v37, v8, v125\n"
    "ds_bpermute_b32 v38, v8, v126\n"
    "ds_bpermute_b32 v39, v8, v127\n"
    "s_waitcnt lgkmcnt(0)\n"
    "v_lshlrev_b32 v8, 16, v12\n"
    "v_and_b32 v9, 0xffff0000, v12\n"
    "v_lshlrev_b32 v10, 16, v13\n"
    "v_and_b32 v11, 0xffff0000, v13\n"
    "v_lshlrev_b32 v12, 16, v14\n"
    "v_and_b32 v13, 0xffff0000, v14\n"
    "v_lshlrev_b32 v14, 16, v15\n"
    "v_and_b32 v15, 0xffff0000, v15\n"
    "v_lshlrev_b32 v16, 16, v20\n"
    "v_and_b32 v17, 0xffff0000, v20\n"
    "v_lshlrev_b32 v18, 16, v21\n"
    "v_and_b32 v19, 0xffff0000, v21\n"
    "v_lshlrev_b32 v20, 16, v22\n"
    "v_and_b32 v21, 0xffff0000, v22\n"
    "v_lshlrev_b32 v22, 16, v23\n"
    "v_and_b32 v23, 0xffff0000, v23\n"
    "v_lshlrev_b32 v24, 16, v28\n"
    "v_and_b32 v25, 0xffff0000, v28\n"
    "v_lshlrev_b32 v26, 16, v29\n"
    "v_and_b32 v27, 0xffff0000, v29\n"
    "v_lshlrev_b32 v28, 16, v30\n"
    "v_and_b32 v29, 0xffff0000, v30\n"
    "v_lshlrev_b32 v30, 16, v31\n"
    "v_and_b32 v31, 0xffff0000, v31\n"
    "v_lshlrev_b32 v32, 16, v36\n"
    "v_and_b32 v33, 0xffff0000, v36\n"
    "v_lshlrev_b32 v34, 16, v37\n"
    "v_and_b32 v35, 0xffff0000, v37\n"
    "v_lshlrev_b32 v36, 16, v38\n"
    "v_and_b32 v37, 0xffff0000, v38\n"
    "v_lshlrev_b32 v38, 16, v39\n"
    "v_and_b32 v39, 0xffff0000, v39\n"
    "v_mov_b32 v4, v140\n"
    "v_mov_b32 v5, v140\n"
    "v_mov_b32 v6, v140\n"
    "v_mov_b32 v7, v140\n"
    "s_barrier\n"
    "s_mov_b32 s68, 135168\n"
    "s_mov_b32 s48, 3\n"
    "s_nop 4\n"
    "s_branch L_WQ_GEMV\n"
    "L_RET3:\n"
    "s_mov_b32 s48, 0\n"
    "v_mov_b32 v4, v136\n"
    "v_mov_b32 v5, v137\n"
    "v_mov_b32 v6, v138\n"
    "v_mov_b32 v7, v139\n"
    "s_add_u32 s61, s49, 16384\n"
    "s_and_b32 s61, s61, 0x3ffff\n"
    "s_andn2_b32 s63, s50, 0x1ff\n"
    "s_add_u32 s61, s61, s63\n"
    "s_add_u32 m0, s52, 28672\n"
    "s_add_u32 s63, s61, 0\n"
    "buffer_load_dwordx4 v128, s[8:11], s63 offen offset:0 nt lds\n"
    "buffer_load_dwordx4 v129, s[8:11], s63 offen offset:1024 nt lds\n"
    "buffer_load_dwordx4 v130, s[8:11], s63 offen offset:2048 nt lds\n"
    "buffer_load_dwordx4 v131, s[8:11], s63 offen offset:3072 nt lds\n"
    "s_add_u32 m0, s52, 32768\n"
    "s_add_u32 s63, s61, 4096\n"
    "buffer_load_dwordx4 v132, s[8:11], s63 offen offset:0 nt lds\n"
    "buffer_load_dwordx4 v133, s[8:11], s63 offen offset:1024 nt lds\n"
    "buffer_load_dwordx4 v134, s[8:11], s63 offen offset:2048 nt lds\n"
    "buffer_load_dwordx4 v135, s[8:11], s63 offen offset:3072 nt lds\n"
    "v_and_b32 v44, 15, v0\n"
    "v_lshrrev_b32 v45, 4, v0\n"
    "v_xor_b32 v45, v45, v44\n"
    "v_lshlrev_b32 v45, 4, v45\n"
    "v_lshl_or_b32 v44, v44, 9, v45\n"
    "v_add_u32 v40, s52, v44\n"
    "v_xor_b32 v41, 64, v40\n"
    "v_xor_b32 v42, 128, v40\n"
    "v_xor_b32 v43, 192, v40\n"
    "v_lshrrev_b32 v44, 2, v0\n"
    "v_lshlrev_b32 v44, 7, v44\n"
    "v_bfe_u32 v45, v0, 3, 2\n"
    "v_lshl_or_b32 v44, v45, 5, v44\n"
    "v_and_b32 v45, 3, v0\n"
    "v_lshl_or_b32 v44, v45, 3, v44\n"
    "v_add_u32 v56, s52, v44\n"
    "v_xor_b32 v57, 32, v56\n"
    "v_xor_b32 v58, 64, v56\n"
    "v_xor_b32 v59, 96, v56\n"
    "s_waitcnt vmcnt(16)\n"
    "ds_read_b128 a[0:3], v40 offset:0\n"
    "ds_read_b128 a[4:7], v41 offset:0\n"
    "ds_read_b128 a[8:11], v42 offset:0\n"
    "ds_read_b128 a[12:15], v43 offset:0\n"
    "ds_read_b128 a[16:19], v40 offset:256\n"
    "ds_read_b128 a[20:23], v41 offset:256\n"
    "ds_read_b128 a[24:27], v42 offset:256\n"
    "ds_read_b128 a[28:31], v43 offset:256\n"
    "s_waitcnt lgkmcnt(0)\n"
    "s_add_u32 s61, s49, 24576\n"
    "s_and_b32 s61, s61, 0x3ffff\n"
    "s_andn2_b32 s63, s50, 0x1ff\n"
    "s_add_u32 s61, s61, s63\n"
    "s_add_u32 m0, s52, 0\n"
    "s_add_u32 s63, s61, 0\n"
    "buffer_load_dwordx4 v128, s[8:11], s63 offen offset:0 nt lds\n"
    "buffer_load_dwordx4 v129, s[8:11], s63 offen offset:1024 nt lds\n"
    "buffer_load_dwordx4 v130, s[8:11], s63 offen offset:2048 nt lds\n"
    "buffer_load_dwordx4 v131, s[8:11], s63 offen offset:3072 nt lds\n"
    "s_add_u32 m0, s52, 4096\n"
    "s_add_u32 s63, s61, 4096\n"
    "buffer_load_dwordx4 v132, s[8:11], s63 offen offset:0 nt lds\n"
    "buffer_load_dwordx4 v133, s[8:11], s63 offen offset:1024 nt lds\n"
    "buffer_load_dwordx4 v134, s[8:11], s63 offen offset:2048 nt lds\n"
    "buffer_load_dwordx4 v135, s[8:11], s63 offen offset:3072 nt lds\n"
    "s_waitcnt vmcnt(16)\n"
    "ds_read_b128 a[32:35], v40 offset:8192\n"
    "ds_read_b128 a[36:39], v41 offset:8192\n"
    "ds_read_b128 a[40:43], v42 offset:8192\n"
    "ds_read_b128 a[44:47], v43 offset:8192\n"
    "ds_read_b128 a[48:51], v40 offset:8448\n"
    "ds_read_b128 a[52:55], v41 offset:8448\n"
    "ds_read_b128 a[56:59], v42 offset:8448\n"
    "ds_read_b128 a[60:63], v43 offset:8448\n"
    "s_waitcnt lgkmcnt(0)\n"
    "s_cmp_lg_u32 s44, 0\n"
    "s_cbranch_scc1 L_NORDY\n"
    "s_waitcnt vmcnt(16)\n"
    "v_mov_b32 v8, s64\n"
    "v_mov_b32 v9, s65\n"
    "s_lshl_b32 s54, s2, 3\n"
    "s_add_u32 s54, s54, 4096\n"
    "v_mov_b32 v10, 0\n"
    "s_mov_b64 exec, 1\n"
    "buffer_store_dwordx2 v[8:9], v10, s[4:7], s54 offen sc1\n"
    "s_mov_b64 exec, -1\n"
    "L_NORDY:\n"
    "s_add_u32 s61, s49, 32768\n"
    "s_and_b32 s61, s61, 0x3ffff\n"
    "s_andn2_b32 s63, s50, 0x1ff\n"
    "s_add_u32 s61, s61, s63\n"
    "s_add_u32 m0, s52, 8192\n"
    "s_add_u32 s63, s61, 0\n"
    "buffer_load_dwordx4 v128, s[8:11], s63 offen offset:0 nt lds\n"
    "buffer_load_dwordx4 v129, s[8:11], s63 offen offset:1024 nt lds\n"
    "buffer_load_dwordx4 v130, s[8:11], s63 offen offset:2048 nt lds\n"
    "buffer_load_dwordx4 v131, s[8:11], s63 offen offset:3072 nt lds\n"
    "s_add_u32 m0, s52, 12288\n"
    "s_add_u32 s63, s61, 4096\n"
    "buffer_load_dwordx4 v132, s[8:11], s63 offen offset:0 nt lds\n"
    "buffer_load_dwordx4 v133, s[8:11], s63 offen offset:1024 nt lds\n"
    "buffer_load_dwordx4 v134, s[8:11], s63 offen offset:2048 nt lds\n"
    "buffer_load_dwordx4 v135, s[8:11], s63 offen offset:3072 nt lds\n"
    "s_waitcnt vmcnt(16)\n"
    "ds_read_b128 a[64:67], v40 offset:28672\n"
    "ds_read_b128 a[68:71], v41 offset:28672\n"
    "ds_read_b128 a[72:75], v42 offset:28672\n"
    "ds_read_b128 a[76:79], v43 offset:28672\n"
    "ds_read_b128 a[80:83], v40 offset:28928\n"
    "ds_read_b128 a[84:87], v41 offset:28928\n"
    "ds_read_b128 a[88:91], v42 offset:28928\n"
    "ds_read_b128 a[92:95], v43 offset:28928\n"
    "s_waitcnt lgkmcnt(0)\n"
    "s_add_u32 s61, s49, 40960\n"
    "s_and_b32 s61, s61, 0x3ffff\n"
    "s_andn2_b32 s63, s50, 0x1ff\n"
    "s_add_u32 s61, s61, s63\n"
    "s_add_u32 m0, s52, 28672\n"
    "s_add_u32 s63, s61, 0\n"
    "buffer_load_dwordx4 v128, s[8:11], s63 offen offset:0 nt lds\n"
    "buffer_load_dwordx4 v129, s[8:11], s63 offen offset:1024 nt lds\n"
    "buffer_load_dwordx4 v130, s[8:11], s63 offen offset:2048 nt lds\n"
    "buffer_load_dwordx4 v131, s[8:11], s63 offen offset:3072 nt lds\n"
    "s_add_u32 m0, s52, 32768\n"
    "s_add_u32 s63, s61, 4096\n"
    "buffer_load_dwordx4 v132, s[8:11], s63 offen offset:0 nt lds\n"
    "buffer_load_dwordx4 v133, s[8:11], s63 offen offset:1024 nt lds\n"
    "buffer_load_dwordx4 v134, s[8:11], s63 offen offset:2048 nt lds\n"
    "buffer_load_dwordx4 v135, s[8:11], s63 offen offset:3072 nt lds\n"
    "s_waitcnt vmcnt(16)\n"
    "ds_read_b128 a[96:99], v40 offset:0\n"
    "ds_read_b128 a[100:103], v41 offset:0\n"
    "ds_read_b128 a[104:107], v42 offset:0\n"
    "ds_read_b128 a[108:111], v43 offset:0\n"
    "ds_read_b128 a[112:115], v40 offset:256\n"
    "ds_read_b128 a[116:119], v41 offset:256\n"
    "ds_read_b128 a[120:123], v42 offset:256\n"
    "ds_read_b128 a[124:127], v43 offset:256\n"
    "s_waitcnt lgkmcnt(0)\n"
    "s_add_u32 s61, s49, 49152\n"
    "s_and_b32 s61, s61, 0x3ffff\n"
    "s_andn2_b32 s63, s50, 0x1ff\n"
    "s_add_u32 s61, s61, s63\n"
    "s_add_u32 m0, s52, 0\n"
    "s_add_u32 s63, s61, 0\n"
    "buffer_load_dwordx4 v128, s[8:11], s63 offen offset:0 nt lds\n"
    "buffer_load_dwordx4 v129, s[8:11], s63 offen offset:1024 nt lds\n"
    "buffer_load_dwordx4 v130, s[8:11], s63 offen offset:2048 nt lds\n"
    "buffer_load_dwordx4 v131, s[8:11], s63 offen offset:3072 nt lds\n"
    "s_add_u32 m0, s52, 4096\n"
    "s_add_u32 s63, s61, 4096\n"
    "buffer_load_dwordx4 v132, s[8:11], s63 offen offset:0 nt lds\n"
    "buffer_load_dwordx4 v133, s[8:11], s63 offen offset:1024 nt lds\n"
    "buffer_load_dwordx4 v134, s[8:11], s63 offen offset:2048 nt lds\n"
    "buffer_load_dwordx4 v135, s[8:11], s63 offen offset:3072 nt lds\n"
    "s_waitcnt vmcnt(16)\n"
    "ds_read_b128 a[128:131], v40 offset:8192\n"
    "ds_read_b128 a[132:135], v41 offset:8192\n"
    "ds_read_b128 a[136:139], v42 offset:8192\n"
    "ds_read_b128 a[140:143], v43 offset:8192\n"
    "ds_read_b128 a[144:147], v40 offset:8448\n"
    "ds_read_b128 a[148:151], v41 offset:8448\n"
    "ds_read_b128 a[152:155], v42 offset:8448\n"
    "ds_read_b128 a[156:159], v43 offset:8448\n"
    "s_waitcnt lgkmcnt(0)\n"
    "v_lshlrev_b32 v46, 5, v0\n"
    "s_mov_b32 s54, 4096\n"
    "buffer_load_dwordx4 v[48:51], v46, s[4:7], s54 offen sc1\n"
    "buffer_load_dwordx4 v[52:55], v46, s[4:7], s54 offen offset:16 sc1\n"
    "s_add_u32 s61, s49, 57344\n"
    "s_and_b32 s61, s61, 0x3ffff\n"
    "s_andn2_b32 s63, s50, 0x1ff\n"
    "s_add_u32 s61, s61, s63\n"
    "s_add_u32 m0, s52, 8192\n"
    "s_add_u32 s63, s61, 0\n"
    "buffer_load_dwordx4 v128, s[8:11], s63 offen offset:0 nt lds\n"
    "buffer_load_dwordx4 v129, s[8:11], s63 offen offset:1024 nt lds\n"
    "buffer_load_dwordx4 v130, s[8:11], s63 offen offset:2048 nt lds\n"
    "buffer_load_dwordx4 v131, s[8:11], s63 offen offset:3072 nt lds\n"
    "s_add_u32 m0, s52, 12288\n"
    "s_add_u32 s63, s61, 4096\n"
    "buffer_load_dwordx4 v132, s[8:11], s63 offen offset:0 nt lds\n"
    "buffer_load_dwordx4 v133, s[8:11], s63 offen offset:1024 nt lds\n"
    "buffer_load_dwordx4 v134, s[8:11], s63 offen offset:2048 nt lds\n"
    "buffer_load_dwordx4 v135, s[8:11], s63 offen offset:3072 nt lds\n"
    "s_waitcnt vmcnt(18)\n"
    "ds_read_b128 a[160:163], v40 offset:28672\n"
    "ds_read_b128 a[164:167], v41 offset:28672\n"
    "ds_read_b128 a[168:171], v42 offset:28672\n"
    "ds_read_b128 a[172:175], v43 offset:28672\n"
    "ds_read_b128 a[176:179], v40 offset:28928\n"
    "ds_read_b128 a[180:183], v41 offset:28928\n"
    "ds_read_b128 a[184:187], v42 offset:28928\n"
    "ds_read_b128 a[188:191], v43 offset:28928\n"
    "s_waitcnt lgkmcnt(0)\n"
    "s_add_u32 s61, s1, 0\n"
    "s_and_b32 s61, s61, 0x3ffff\n"
    "s_add_u32 s61, s61, s50\n"
    "s_add_u32 m0, s52, 28672\n"
    "s_add_u32 s63, s61, 0\n"
    "buffer_load_dwordx4 v1, s[12:15], s63 offen offset:0 nt lds\n"
    "s_add_u32 s63, s61, 3072\n"
    "buffer_load_dwordx4 v1, s[12:15], s63 offen offset:1024 nt lds\n"
    "s_add_u32 s63, s61, 6144\n"
    "buffer_load_dwordx4 v1, s[12:15], s63 offen offset:2048 nt lds\n"
    "s_add_u32 s63, s61, 9216\n"
    "buffer_load_dwordx4 v1, s[12:15], s63 offen offset:3072 nt lds\n"
    "s_add_u32 s61, s1, 16384\n"
    "s_and_b32 s61, s61, 0x3ffff\n"
    "s_add_u32 s61, s61, s50\n"
    "s_add_u32 m0, s52, 32768\n"
    "s_add_u32 s63, s61, 0\n"
    "buffer_load_dwordx4 v1, s[12:15], s63 offen offset:0 nt lds\n"
    "s_add_u32 s63, s61, 3072\n"
    "buffer_load_dwordx4 v1, s[12:15], s63 offen offset:1024 nt lds\n"
    "s_add_u32 s63, s61, 6144\n"
    "buffer_load_dwordx4 v1, s[12:15], s63 offen offset:2048 nt lds\n"
    "s_add_u32 s63, s61, 9216\n"
    "buffer_load_dwordx4 v1, s[12:15], s63 offen offset:3072 nt lds\n"
    "s_waitcnt vmcnt(18)\n"
    "ds_read_b128 a[192:195], v40 offset:0\n"
    "ds_read_b128 a[196:199], v41 offset:0\n"
    "ds_read_b128 a[200:203], v42 offset:0\n"
    "ds_read_b128 a[204:207], v43 offset:0\n"
    "ds_read_b128 a[208:211], v40 offset:256\n"
    "ds_read_b128 a[212:215], v41 offset:256\n"
    "ds_read_b128 a[216:219], v42 offset:256\n"
    "ds_read_b128 a[220:223], v43 offset:256\n"
    "s_waitcnt lgkmcnt(0)\n"
    "s_waitcnt vmcnt(16)\n"
    "L_FLCHK:\n"
    "v_cmp_ne_u64 vcc, s[64:65], v[48:49]\n"
    "v_cmp_ne_u64 s[56:57], s[64:65], v[50:51]\n"
    "v_cmp_ne_u64 s[58:59], s[64:65], v[52:53]\n"
    "v_cmp_ne_u64 s[60:61], s[64:65], v[54:55]\n"
    "s_or_b64 vcc, vcc, s[56:57]\n"
    "s_or_b64 s[58:59], s[58:59], s[60:61]\n"
    "s_or_b64 vcc, vcc, s[58:59]\n"
    "s_cbranch_scc0 L_FLOK\n"
    "s_sub_u32 s73, s73, 1\n"
    "s_cbranch_scc1 L_FLTMO\n"
    "s_sleep 8\n"
    "s_mov_b32 s54, 4096\n"
    "buffer_load_dwordx4 v[48:51], v46, s[4:7], s54 offen sc1\n"
    "buffer_load_dwordx4 v[52:55], v46, s[4:7], s54 offen offset:16 sc1\n"
    "s_waitcnt vmcnt(0)\n"
    "s_branch L_FLCHK\n"
    "L_FLTMO:\n"
    "s_mov_b32 s73, 0\n"
    "L_FLOK:\n"
    "s_or_b32 s54, s2, s44\n"
    "s_cmp_lg_u32 s54, 0\n"
    "s_cbranch_scc1 L_NOEP\n"
    "v_mov_b32 v8, s64\n"
    "v_mov_b32 v9, s65\n"
    "v_mov_b32 v10, 0\n"
    "s_mov_b64 exec, 1\n"
    "buffer_store_dwordx2 v[8:9], v10, s[4:7], 0 offen offset:0 sc1\n"
    "s_mov_b64 exec, -1\n"
    "L_NOEP:\n"
    "s_mov_b32 s70, 204800\n"
    "s_mov_b32 s69, 65536\n"
    "v_lshlrev_b32 v18, 3, v0\n"
    "v_lshlrev_b32 v17, 4, v0\n"
    "s_lshl_b32 s54, s45, 13\n"
    "s_lshl_b32 s55, s44, 9\n"
    "s_add_u32 s54, s54, s55\n"
    "s_add_u32 s54, s54, s69\n"
    "s_add_u32 s55, s54, 4096\n"
    "buffer_load_dwordx2 v[48:49], v18, s[4:7], s54 offen offset:0 sc1\n"
    "buffer_load_dwordx2 v[50:51], v18, s[4:7], s54 offen offset:2048 sc1\n"
    "buffer_load_dwordx2 v[52:53], v18, s[4:7], s55 offen offset:0 sc1\n"
    "buffer_load_dwordx2 v[54:55], v18, s[4:7], s55 offen offset:2048 sc1\n"
    "s_lshl_b32 s54, s45, 13\n"
    "s_lshl_b32 s55, s44, 9\n"
    "s_add_u32 s54, s54, s55\n"
    "s_add_u32 s54, s54, s70\n"
    "s_add_u32 s55, s54, 4096\n"
    "buffer_load_dwordx2 v[8:9], v18, s[4:7], s54 offen offset:0 sc1\n"
    "buffer_load_dwordx2 v[10:11], v18, s[4:7], s54 offen offset:2048 sc1\n"
    "buffer_load_dwordx2 v[12:13], v18, s[4:7], s55 offen offset:0 sc1\n"
    "buffer_load_dwordx2 v[14:15], v18, s[4:7], s55 offen offset:2048 sc1\n"
    "s_lshl_b32 s56, s45, 3\n"
    "s_and_b32 s63, s46, 7\n"
    "s_add_u32 s56, s56, s63\n"
    "s_lshl_b32 s56, s56, 6\n"
    "s_add_u32 s56, s56, s70\n"
    "s_add_u32 s56, s56, 65536\n"
    "s_mov_b64 exec, 15\n"
    "buffer_load_dwordx4 v[28:31], v17, s[4:7], s56 offen sc1\n"
    "s_mov_b64 exec, -1\n"
    "s_add_u32 s61, s1, 32768\n"
    "s_and_b32 s61, s61, 0x3ffff\n"
    "s_add_u32 s61, s61, s50\n"
    "s_add_u32 m0, s52, 0\n"
    "s_add_u32 s63, s61, 0\n"
    "buffer_load_dwordx4 v1, s[12:15], s63 offen offset:0 nt lds\n"
    "s_add_u32 s63, s61, 3072\n"
    "buffer_load_dwordx4 v1, s[12:15], s63 offen offset:1024 nt lds\n"
    "s_add_u32 s63, s61, 6144\n"
    "buffer_load_dwordx4 v1, s[12:15], s63 offen offset:2048 nt lds\n"
    "s_add_u32 s63, s61, 9216\n"
    "buffer_load_dwordx4 v1, s[12:15], s63 offen offset:3072 nt lds\n"
    "s_add_u32 s61, s1, 49152\n"
    "s_and_b32 s61, s61, 0x3ffff\n"
    "s_add_u32 s61, s61, s50\n"
    "s_add_u32 m0, s52, 4096\n"
    "s_add_u32 s63, s61, 0\n"
    "buffer_load_dwordx4 v1, s[12:15], s63 offen offset:0 nt lds\n"
    "s_add_u32 s63, s61, 3072\n"
    "buffer_load_dwordx4 v1, s[12:15], s63 offen offset:1024 nt lds\n"
    "s_add_u32 s63, s61, 6144\n"
    "buffer_load_dwordx4 v1, s[12:15], s63 offen offset:2048 nt lds\n"
    "s_add_u32 s63, s61, 9216\n"
    "buffer_load_dwordx4 v1, s[12:15], s63 offen offset:3072 nt lds\n"
    "s_waitcnt vmcnt(25)\n"
    "ds_read_b128 a[224:227], v40 offset:8192\n"
    "ds_read_b128 a[228:231], v41 offset:8192\n"
    "ds_read_b128 a[232:235], v42 offset:8192\n"
    "ds_read_b128 a[236:239], v43 offset:8192\n"
    "ds_read_b128 a[240:243], v40 offset:8448\n"
    "ds_read_b128 a[244:247], v41 offset:8448\n"
    "ds_read_b128 a[248:251], v42 offset:8448\n"
    "ds_read_b128 a[252:255], v43 offset:8448\n"
    "s_waitcnt lgkmcnt(0)\n"
    "s_add_u32 s61, s1, 65536\n"
    "s_and_b32 s61, s61, 0x3ffff\n"
    "s_add_u32 s61, s61, s50\n"
    "s_add_u32 m0, s52, 8192\n"
    "s_add_u32 s63, s61, 0\n"
    "buffer_load_dwordx4 v1, s[12:15], s63 offen offset:0 nt lds\n"
    "s_add_u32 s63, s61, 3072\n"
    "buffer_load_dwordx4 v1, s[12:15], s63 offen offset:1024 nt lds\n"
    "s_add_u32 s63, s61, 6144\n"
    "buffer_load_dwordx4 v1, s[12:15], s63 offen offset:2048 nt lds\n"
    "s_add_u32 s63, s61, 9216\n"
    "buffer_load_dwordx4 v1, s[12:15], s63 offen offset:3072 nt lds\n"
    "s_add_u32 s61, s1, 81920\n"
    "s_and_b32 s61, s61, 0x3ffff\n"
    "s_add_u32 s61, s61, s50\n"
    "s_add_u32 m0, s52, 12288\n"
    "s_add_u32 s63, s61, 0\n"
    "buffer_load_dwordx4 v1, s[12:15], s63 offen offset:0 nt lds\n"
    "s_add_u32 s63, s61, 3072\n"
    "buffer_load_dwordx4 v1, s[12:15], s63 offen offset:1024 nt lds\n"
    "s_add_u32 s63, s61, 6144\n"
    "buffer_load_dwordx4 v1, s[12:15], s63 offen offset:2048 nt lds\n"
    "s_add_u32 s63, s61, 9216\n"
    "buffer_load_dwordx4 v1, s[12:15], s63 offen offset:3072 nt lds\n"
    "s_waitcnt vmcnt(29)\n"
    "ds_read_b64_tr_b16 v[64:65], v56 offset:28672\n"
    "ds_read_b64_tr_b16 v[66:67], v56 offset:30720\n"
    "ds_read_b64_tr_b16 v[68:69], v57 offset:28672\n"
    "ds_read_b64_tr_b16 v[70:71], v57 offset:30720\n"
    "ds_read_b64_tr_b16 v[72:73], v58 offset:28672\n"
    "ds_read_b64_tr_b16 v[74:75], v58 offset:30720\n"
    "ds_read_b64_tr_b16 v[76:77], v59 offset:28672\n"
    "ds_read_b64_tr_b16 v[78:79], v59 offset:30720\n"
    "s_waitcnt lgkmcnt(0)\n"
    "s_add_u32 s61, s1, 98304\n"
    "s_and_b32 s61, s61, 0x3ffff\n"
    "s_add_u32 s61, s61, s50\n"
    "s_add_u32 m0, s52, 28672\n"
    "s_add_u32 s63, s61, 0\n"
    "buffer_load_dwordx4 v1, s[12:15], s63 offen offset:0 nt lds\n"
    "s_add_u32 s63, s61, 3072\n"
    "buffer_load_dwordx4 v1, s[12:15], s63 offen offset:1024 nt lds\n"
    "s_add_u32 s63, s61, 6144\n"
    "buffer_load_dwordx4 v1, s[12:15], s63 offen offset:2048 nt lds\n"
    "s_add_u32 s63, s61, 9216\n"
    "buffer_load_dwordx4 v1, s[12:15], s63 offen offset:3072 nt lds\n"
    "s_waitcnt vmcnt(29)\n"
    "ds_read_b64_tr_b16 v[80:81], v56 offset:32768\n"
    "ds_read_b64_tr_b16 v[82:83], v56 offset:34816\n"
    "ds_read_b64_tr_b16 v[84:85], v57 offset:32768\n"
    "ds_read_b64_tr_b16 v[86:87], v57 offset:34816\n"
    "ds_read_b64_tr_b16 v[88:89], v58 offset:32768\n"
    "ds_read_b64_tr_b16 v[90:91], v58 offset:34816\n"
    "ds_read_b64_tr_b16 v[92:93], v59 offset:32768\n"
    "ds_read_b64_tr_b16 v[94:95], v59 offset:34816\n"
    "s_waitcnt lgkmcnt(0)\n"
    "s_add_u32 s61, s1, 196608\n"
    "s_and_b32 s61, s61, 0x3ffff\n"
    "s_add_u32 s61, s61, s50\n"
    "s_add_u32 m0, s52, 32768\n"
    "s_add_u32 s63, s61, 0\n"
    "buffer_load_dwordx4 v1, s[12:15], s63 offen offset:0 nt lds\n"
    "s_add_u32 s63, s61, 3072\n"
    "buffer_load_dwordx4 v1, s[12:15], s63 offen offset:1024 nt lds\n"
    "s_add_u32 s63, s61, 6144\n"
    "buffer_load_dwordx4 v1, s[12:15], s63 offen offset:2048 nt lds\n"
    "s_add_u32 s63, s61, 9216\n"
    "buffer_load_dwordx4 v1, s[12:15], s63 offen offset:3072 nt lds\n"
    "s_waitcnt vmcnt(24)\n"
    "v_pk_add_f32 v[48:49], v[48:49], v[50:51]\n"
    "v_pk_add_f32 v[52:53], v[52:53], v[54:55]\n"
    "v_pk_add_f32 v[2:3], v[48:49], v[52:53]\n"
    "s_nop 4\n"
    "s_branch L_ATT_ADDR\n"
    "L_VMID:\n"
    "v_bfe_u32 v44, v0, 1, 2\n"
    "v_bfe_u32 v45, v0, 4, 2\n"
    "v_xor_b32 v44, v44, v45\n"
    "v_lshlrev_b32 v44, 5, v44\n"
    "v_and_b32 v45, 1, v0\n"
    "v_lshl_or_b32 v44, v45, 4, v44\n"
    "v_lshrrev_b32 v45, 3, v0\n"
    "v_lshl_or_b32 v1, v45, 9, v44\n"
    "s_waitcnt vmcnt(20)\n"
    "ds_read_b64_tr_b16 v[96:97], v56 offset:0\n"
    "ds_read_b64_tr_b16 v[98:99], v56 offset:2048\n"
    "ds_read_b64_tr_b16 v[100:101], v57 offset:0\n"
    "ds_read_b64_tr_b16 v[102:103], v57 offset:2048\n"
    "ds_read_b64_tr_b16 v[104:105], v58 offset:0\n"
    "ds_read_b64_tr_b16 v[106:107], v58 offset:2048\n"
    "ds_read_b64_tr_b16 v[108:109], v59 offset:0\n"
    "ds_read_b64_tr_b16 v[110:111], v59 offset:2048\n"
    "s_waitcnt lgkmcnt(0)\n"
    "s_add_u32 s61, s1, 114688\n"
    "s_and_b32 s61, s61, 0x3ffff\n"
    "s_add_u32 s61, s61, s50\n"
    "s_add_u32 m0, s52, 0\n"
    "s_add_u32 s63, s61, 0\n"
    "buffer_load_dwordx4 v1, s[12:15], s63 offen offset:0 nt lds\n"
    "s_add_u32 s63, s61, 3072\n"
    "buffer_load_dwordx4 v1, s[12:15], s63 offen offset:1024 nt lds\n"
    "s_add_u32 s63, s61, 6144\n"
    "buffer_load_dwordx4 v1, s[12:15], s63 offen offset:2048 nt lds\n"
    "s_add_u32 s63, s61, 9216\n"
    "buffer_load_dwordx4 v1, s[12:15], s63 offen offset:3072 nt lds\n"
    "s_waitcnt vmcnt(20)\n"
    "ds_read_b64_tr_b16 v[112:113], v56 offset:4096\n"
    "ds_read_b64_tr_b16 v[114:115], v56 offset:6144\n"
    "ds_read_b64_tr_b16 v[116:117], v57 offset:4096\n"
    "ds_read_b64_tr_b16 v[118:119], v57 offset:6144\n"
    "ds_read_b64_tr_b16 v[120:121], v58 offset:4096\n"
    "ds_read_b64_tr_b16 v[122:123], v58 offset:6144\n"
    "ds_read_b64_tr_b16 v[124:125], v59 offset:4096\n"
    "ds_read_b64_tr_b16 v[126:127], v59 offset:6144\n"
    "s_waitcnt lgkmcnt(0)\n"
    "s_add_u32 s61, s1, 131072\n"
    "s_and_b32 s61, s61, 0x3ffff\n"
    "s_add_u32 s61, s61, s50\n"
    "s_add_u32 m0, s52, 4096\n"
    "s_add_u32 s63, s61, 0\n"
    "buffer_load_dwordx4 v1, s[12:15], s63 offen offset:0 nt lds\n"
    "s_add_u32 s63, s61, 3072\n"
    "buffer_load_dwordx4 v1, s[12:15], s63 offen offset:1024 nt lds\n"
    "s_add_u32 s63, s61, 6144\n"
    "buffer_load_dwordx4 v1, s[12:15], s63 offen offset:2048 nt lds\n"
    "s_add_u32 s63, s61, 9216\n"
    "buffer_load_dwordx4 v1, s[12:15], s63 offen offset:3072 nt lds\n"
    "s_waitcnt vmcnt(20)\n"
    "ds_read_b64_tr_b16 v[128:129], v56 offset:8192\n"
    "ds_read_b64_tr_b16 v[130:131], v56 offset:10240\n"
    "ds_read_b64_tr_b16 v[132:133], v57 offset:8192\n"
    "ds_read_b64_tr_b16 v[134:135], v57 offset:10240\n"
    "ds_read_b64_tr_b16 v[136:137], v58 offset:8192\n"
    "ds_read_b64_tr_b16 v[138:139], v58 offset:10240\n"
    "ds_read_b64_tr_b16 v[140:141], v59 offset:8192\n"
    "ds_read_b64_tr_b16 v[142:143], v59 offset:10240\n"
    "s_waitcnt lgkmcnt(0)\n"
    "s_add_u32 s61, s1, 147456\n"
    "s_and_b32 s61, s61, 0x3ffff\n"
    "s_add_u32 s61, s61, s50\n"
    "s_add_u32 m0, s52, 8192\n"
    "s_add_u32 s63, s61, 0\n"
    "buffer_load_dwordx4 v1, s[12:15], s63 offen offset:0 nt lds\n"
    "s_add_u32 s63, s61, 3072\n"
    "buffer_load_dwordx4 v1, s[12:15], s63 offen offset:1024 nt lds\n"
    "s_add_u32 s63, s61, 6144\n"
    "buffer_load_dwordx4 v1, s[12:15], s63 offen offset:2048 nt lds\n"
    "s_add_u32 s63, s61, 9216\n"
    "buffer_load_dwordx4 v1, s[12:15], s63 offen offset:3072 nt lds\n"
    "s_waitcnt vmcnt(20)\n"
    "ds_read_b64_tr_b16 v[144:145], v56 offset:12288\n"
    "ds_read_b64_tr_b16 v[146:147], v56 offset:14336\n"
    "ds_read_b64_tr_b16 v[148:149], v57 offset:12288\n"
    "ds_read_b64_tr_b16 v[150:151], v57 offset:14336\n"
    "ds_read_b64_tr_b16 v[152:153], v58 offset:12288\n"
    "ds_read_b64_tr_b16 v[154:155], v58 offset:14336\n"
    "ds_read_b64_tr_b16 v[156:157], v59 offset:12288\n"
    "ds_read_b64_tr_b16 v[158:159], v59 offset:14336\n"
    "s_waitcnt lgkmcnt(0)\n"
    "s_add_u32 s61, s1, 163840\n"
    "s_and_b32 s61, s61, 0x3ffff\n"
    "s_add_u32 s61, s61, s50\n"
    "s_add_u32 m0, s52, 12288\n"
    "s_add_u32 s63, s61, 0\n"
    "buffer_load_dwordx4 v1, s[12:15], s63 offen offset:0 nt lds\n"
    "s_add_u32 s63, s61, 3072\n"
    "buffer_load_dwordx4 v1, s[12:15], s63 offen offset:1024 nt lds\n"
    "s_add_u32 s63, s61, 6144\n"
    "buffer_load_dwordx4 v1, s[12:15], s63 offen offset:2048 nt lds\n"
    "s_add_u32 s63, s61, 9216\n"
    "buffer_load_dwordx4 v1, s[12:15], s63 offen offset:3072 nt lds\n"
    "s_nop 4\n"
    "s_branch L_PV_A\n"
    "L_VREST:\n"
    "s_waitcnt vmcnt(21)\n"
    "ds_read_b64_tr_b16 v[160:161], v56 offset:28672\n"
    "ds_read_b64_tr_b16 v[162:163], v56 offset:30720\n"
    "ds_read_b64_tr_b16 v[164:165], v57 offset:28672\n"
    "ds_read_b64_tr_b16 v[166:167], v57 offset:30720\n"
    "ds_read_b64_tr_b16 v[168:169], v58 offset:28672\n"
    "ds_read_b64_tr_b16 v[170:171], v58 offset:30720\n"
    "ds_read_b64_tr_b16 v[172:173], v59 offset:28672\n"
    "ds_read_b64_tr_b16 v[174:175], v59 offset:30720\n"
    "s_waitcnt lgkmcnt(0)\n"
    "s_add_u32 s61, s1, 180224\n"
    "s_and_b32 s61, s61, 0x3ffff\n"
    "s_add_u32 s61, s61, s50\n"
    "s_add_u32 m0, s52, 28672\n"
    "s_add_u32 s63, s61, 0\n"
    "buffer_load_dwordx4 v1, s[12:15], s63 offen offset:0 nt lds\n"
    "s_add_u32 s63, s61, 3072\n"
    "buffer_load_dwordx4 v1, s[12:15], s63 offen offset:1024 nt lds\n"
    "s_add_u32 s63, s61, 6144\n"
    "buffer_load_dwordx4 v1, s[12:15], s63 offen offset:2048 nt lds\n"
    "s_add_u32 s63, s61, 9216\n"
    "buffer_load_dwordx4 v1, s[12:15], s63 offen offset:3072 nt lds\n"
    "s_mov_b64 s[8:9], s[28:29]\n"
    "s_mov_b64 s[12:13], s[32:33]\n"
    "s_add_u32 m0, s52, 16384\n"
    "s_add_u32 s63, s51, 0\n"
    "buffer_load_dwordx4 v1, s[12:15], s63 offen offset:0 nt lds\n"
    "s_add_u32 s63, s51, 3072\n"
    "buffer_load_dwordx4 v1, s[12:15], s63 offen offset:1024 nt lds\n"
    "s_add_u32 s63, s51, 6144\n"
    "buffer_load_dwordx4 v1, s[12:15], s63 offen offset:2048 nt lds\n"
    "s_add_u32 s63, s51, 9216\n"
    "buffer_load_dwordx4 v1, s[12:15], s63 offen offset:3072 nt lds\n"
    "s_add_u32 m0, s52, 20480\n"
    "s_add_u32 s63, s51, 16384\n"
    "buffer_load_dwordx4 v1, s[12:15], s63 offen offset:0 nt lds\n"
    "s_add_u32 s63, s51, 19456\n"
    "buffer_load_dwordx4 v1, s[12:15], s63 offen offset:1024 nt lds\n"
    "s_add_u32 s63, s51, 22528\n"
    "buffer_load_dwordx4 v1, s[12:15], s63 offen offset:2048 nt lds\n"
    "s_add_u32 s63, s51, 25600\n"
    "buffer_load_dwordx4 v1, s[12:15], s63 offen offset:3072 nt lds\n"
    "s_add_u32 m0, s52, 24576\n"
    "s_add_u32 s63, s51, 32768\n"
    "buffer_load_dwordx4 v1, s[12:15], s63 offen offset:0 nt lds\n"
    "s_add_u32 s63, s51, 35840\n"
    "buffer_load_dwordx4 v1, s[12:15], s63 offen offset:1024 nt lds\n"
    "s_add_u32 s63, s51, 38912\n"
    "buffer_load_dwordx4 v1, s[12:15], s63 offen offset:2048 nt lds\n"
    "s_add_u32 s63, s51, 41984\n"
    "buffer_load_dwordx4 v1, s[12:15], s63 offen offset:3072 nt lds\n"
    "s_waitcnt vmcnt(29)\n"
    "ds_read_b64_tr_b16 v[176:177], v56 offset:0\n"
    "ds_read_b64_tr_b16 v[178:179], v56 offset:2048\n"
    "ds_read_b64_tr_b16 v[180:181], v57 offset:0\n"
    "ds_read_b64_tr_b16 v[182:183], v57 offset:2048\n"
    "ds_read_b64_tr_b16 v[184:185], v58 offset:0\n"
    "ds_read_b64_tr_b16 v[186:187], v58 offset:2048\n"
    "ds_read_b64_tr_b16 v[188:189], v59 offset:0\n"
    "ds_read_b64_tr_b16 v[190:191], v59 offset:2048\n"
    "s_waitcnt lgkmcnt(0)\n"
    "s_waitcnt vmcnt(25)\n"
    "ds_read_b64_tr_b16 v[192:193], v56 offset:4096\n"
    "ds_read_b64_tr_b16 v[194:195], v56 offset:6144\n"
    "ds_read_b64_tr_b16 v[196:197], v57 offset:4096\n"
    "ds_read_b64_tr_b16 v[198:199], v57 offset:6144\n"
    "ds_read_b64_tr_b16 v[200:201], v58 offset:4096\n"
    "ds_read_b64_tr_b16 v[202:203], v58 offset:6144\n"
    "ds_read_b64_tr_b16 v[204:205], v59 offset:4096\n"
    "ds_read_b64_tr_b16 v[206:207], v59 offset:6144\n"
    "s_waitcnt lgkmcnt(0)\n"
    "s_waitcnt vmcnt(21)\n"
    "ds_read_b64_tr_b16 v[208:209], v56 offset:8192\n"
    "ds_read_b64_tr_b16 v[210:211], v56 offset:10240\n"
    "ds_read_b64_tr_b16 v[212:213], v57 offset:8192\n"
    "ds_read_b64_tr_b16 v[214:215], v57 offset:10240\n"
    "ds_read_b64_tr_b16 v[216:217], v58 offset:8192\n"
    "ds_read_b64_tr_b16 v[218:219], v58 offset:10240\n"
    "ds_read_b64_tr_b16 v[220:221], v59 offset:8192\n"
    "ds_read_b64_tr_b16 v[222:223], v59 offset:10240\n"
    "s_waitcnt lgkmcnt(0)\n"
    "s_waitcnt vmcnt(17)\n"
    "ds_read_b64_tr_b16 v[224:225], v56 offset:12288\n"
    "ds_read_b64_tr_b16 v[226:227], v56 offset:14336\n"
    "ds_read_b64_tr_b16 v[228:229], v57 offset:12288\n"
    "ds_read_b64_tr_b16 v[230:231], v57 offset:14336\n"
    "ds_read_b64_tr_b16 v[232:233], v58 offset:12288\n"
    "ds_read_b64_tr_b16 v[234:235], v58 offset:14336\n"
    "ds_read_b64_tr_b16 v[236:237], v59 offset:12288\n"
    "ds_read_b64_tr_b16 v[238:239], v59 offset:14336\n"
    "s_waitcnt lgkmcnt(0)\n"
    "s_waitcnt vmcnt(12)\n"
    "ds_read_b64_tr_b16 v[240:241], v56 offset:28672\n"
    "ds_read_b64_tr_b16 v[242:243], v56 offset:30720\n"
    "ds_read_b64_tr_b16 v[244:245], v57 offset:28672\n"
    "ds_read_b64_tr_b16 v[246:247], v57 offset:30720\n"
    "ds_read_b64_tr_b16 v[248:249], v58 offset:28672\n"
    "ds_read_b64_tr_b16 v[250:251], v58 offset:30720\n"
    "ds_read_b64_tr_b16 v[252:253], v59 offset:28672\n"
    "ds_read_b64_tr_b16 v[254:255], v59 offset:30720\n"
    "s_waitcnt lgkmcnt(0)\n"
    "s_memrealtime s[62:63]\n"
    "s_waitcnt lgkmcnt(0)\n"
    "s_sub_u32 s3, s62, 4000\n"
    "s_nop 4\n"
    "s_branch L_PV_A2\n"
    "L_Q:\n"
    "s_memrealtime s[64:65]\n"
    "v_lshlrev_b32 v18, 3, v0\n"
    "v_lshlrev_b32 v17, 4, v0\n"
    "s_lshl_b32 s54, s45, 13\n"
    "s_lshl_b32 s55, s44, 9\n"
    "s_add_u32 s54, s54, s55\n"
    "s_add_u32 s54, s54, s70\n"
    "s_add_u32 s55, s54, 4096\n"
    "s_lshl_b32 s56, s45, 3\n"
    "s_and_b32 s63, s46, 7\n"
    "s_add_u32 s56, s56, s63\n"
    "s_lshl_b32 s56, s56, 6\n"
    "s_add_u32 s56, s56, s70\n"
    "s_add_u32 s56, s56, 65536\n"
    "s_waitcnt lgkmcnt(0)\n"
    "s_add_u32 s67, s64, s82\n"
    "s_sub_u32 s62, s3, s67\n"
    "s_bfe_i32 s62, s62, 1310720\n"
    "s_sub_u32 s62, s62, 1\n"
    "s_cmp_lt_u32 s62, 600\n"
    "s_cselect_b32 s67, s3, s67\n"
    "L_w1:\n"
    "s_memrealtime s[62:63]\n"
    "s_waitcnt lgkmcnt(0)\n"
    "s_sub_u32 s62, s67, s62\n"
    "s_bfe_i32 s62, s62, 1310720\n"
    "s_cmp_gt_i32 s62, 0\n"
    "s_cbranch_scc1 L_w1\n"
    "buffer_load_dwordx2 v[8:9], v18, s[4:7], s54 offen offset:0 sc1\n"
    "buffer_load_dwordx2 v[10:11], v18, s[4:7], s54 offen offset:2048 sc1\n"
    "buffer_load_dwordx2 v[12:13], v18, s[4:7], s55 offen offset:0 sc1\n"
    "buffer_load_dwordx2 v[14:15], v18, s[4:7], s55 offen offset:2048 sc1\n"
    "s_mov_b64 exec, 15\n"
    "buffer_load_dwordx4 v[28:31], v17, s[4:7], s56 offen sc1\n"
    "s_mov_b64 exec, -1\n"
    "L_ATT_ADDR:\n"
    "v_lshrrev_b32 v44, 2, v0\n"
    "v_lshlrev_b32 v44, 7, v44\n"
    "v_bfe_u32 v45, v0, 3, 2\n"
    "v_lshl_or_b32 v44, v45, 5, v44\n"
    "v_and_b32 v45, 3, v0\n"
    "v_lshl_or_b32 v44, v45, 3, v44\n"
    "v_add_u32 v56, s52, v44\n"
    "v_xor_b32 v57, 32, v56\n"
    "v_xor_b32 v58, 64, v56\n"
    "v_xor_b32 v59, 96, v56\n"
    "v_and_b32 v44, 3, v0\n"
    "v_lshrrev_b32 v45, 4, v0\n"
    "v_lshlrev_b32 v45, 4, v45\n"
    "v_mul_u32_u24 v63, 576, v44\n"
    "v_add_u32 v63, v63, v45\n"
    "v_add_u32 v63, 1024, v63\n"
    "v_lshl_or_b32 v46, v44, 6, v45\n"
    "v_bfe_u32 v47, v0, 2, 2\n"
    "v_lshl_or_b32 v47, v47, 10, v46\n"
    "v_add_u32 v47, 5632, v47\n"
    "s_mov_b64 s[58:59], s[74:75]\n"
    "v_cndmask_b32_e64 v52, 0, v47, s[58:59]\n"
    "v_cndmask_b32_e64 v48, 0, v63, s[58:59]\n"
    "s_lshl_b64 s[58:59], s[58:59], 4\n"
    "v_cndmask_b32_e64 v53, 0, v47, s[58:59]\n"
    "v_cndmask_b32_e64 v49, 0, v63, s[58:59]\n"
    "s_lshl_b64 s[58:59], s[58:59], 4\n"
    "v_cndmask_b32_e64 v54, 0, v47, s[58:59]\n"
    "v_cndmask_b32_e64 v50, 0, v63, s[58:59]\n"
    "s_lshl_b64 s[58:59], s[58:59], 4\n"
    "v_cndmask_b32_e64 v55, 0, v47, s[58:59]\n"
    "v_cndmask_b32_e64 v51, 0, v63, s[58:59]\n"
    "s_lshl_b32 s60, s44, 10\n"
    "s_add_u32 s60, s60, 5632\n"
    "v_add_u32 v60, s60, v46\n"
    "v_and_b32 v44, 2, v0\n"
    "v_cmp_ne_u32 vcc, 0, v44\n"
    "v_mov_b32 v44, 0xffff0000\n"
    "s_nop 0\n"
    "v_cndmask_b32_e64 v61, 0, v44, vcc\n"
    "v_and_b32 v44, 1, v0\n"
    "v_lshlrev_b32 v44, 3, v44\n"
    "s_lshl_b32 s60, s44, 4\n"
    "s_add_u32 s60, s60, 3328\n"
    "v_add_u32 v62, s60, v44\n"
    "s_lshr_b32 s60, s44, 1\n"
    "s_mul_i32 s60, s60, 576\n"
    "s_and_b32 s61, s44, 1\n"
    "s_lshl_b32 s61, s61, 8\n"
    "s_add_u32 s60, s60, s61\n"
    "s_add_u32 s60, s60, 1024\n"
    "v_lshlrev_b32 v19, 2, v0\n"
    "v_add_u32 v19, s60, v19\n"
    "s_bitcmp1_b32 s47, 0\n"
    "s_cbranch_scc0 L_AA_A\n"
    "v_and_b32 v44, 15, v0\n"
    "v_lshrrev_b32 v45, 4, v0\n"
    "v_xor_b32 v45, v45, v44\n"
    "v_lshlrev_b32 v45, 4, v45\n"
    "v_lshl_or_b32 v44, v44, 9, v45\n"
    "v_add_u32 v48, s52, v44\n"
    "v_xor_b32 v49, 64, v48\n"
    "v_xor_b32 v50, 128, v48\n"
    "v_xor_b32 v51, 192, v48\n"
    "s_branch L_AA_X\n"
    "L_AA_A:\n"
    "v_bfe_u32 v44, v0, 3, 1\n"
    "v_bfe_u32 v45, v0, 2, 1\n"
    "v_lshlrev_b32 v44, 8, v44\n"
    "v_lshl_or_b32 v44, v45, 3, v44\n"
    "v_add_u32 v60, v60, v44\n"
    "v_bfe_u32 v44, v0, 1, 2\n"
    "v_bfe_u32 v45, v0, 4, 2\n"
    "v_xor_b32 v44, v44, v45\n"
    "v_lshlrev_b32 v44, 5, v44\n"
    "v_and_b32 v45, 1, v0\n"
    "v_lshl_or_b32 v44, v45, 4, v44\n"
    "v_lshrrev_b32 v45, 3, v0\n"
    "v_lshl_or_b32 v63, v45, 9, v44\n"
    "s_cmp_eq_u32 s47, 0\n"
    "s_cbranch_scc1 L_Q_GOT\n"
    "L_AA_X:\n"
    "s_waitcnt vmcnt(0)\n"
    "L_Q_GOT:\n"
    "v_pk_add_f32 v[20:21], v[8:9], v[10:11]\n"
    "v_pk_add_f32 v[22:23], v[12:13], v[14:15]\n"
    "v_add_f32_dpp v24, v28, v28 quad_perm:[1,0,3,2] row_mask:0xf bank_mask:0xf\n"
    "v_add_f32_dpp v25, v29, v29 quad_perm:[1,0,3,2] row_mask:0xf bank_mask:0xf\n"
    "v_pk_add_f32 v[20:21], v[20:21], v[22:23]\n"
    "v_add_f32_dpp v24, v24, v24 quad_perm:[2,3,0,1] row_mask:0xf bank_mask:0xf\n"
    "v_add_f32_dpp v25, v25, v25 quad_perm:[2,3,0,1] row_mask:0xf bank_mask:0xf\n"
    "v_mov_b32 v26, 0x3b124925\n"
    "v_readfirstlane_b32 s54, v24\n"
    "v_readfirstlane_b32 s55, v25\n"
    "s_nop 0\n"
    "v_mul_f32 v24, s54, v26\n"
    "v_mul_f32 v25, s55, v26\n"
    "v_fma_f32 v20, -v24, v2, v20\n"
    "v_fma_f32 v21, -v24, v3, v21\n"
    "v_fma_f32 v25, -v24, v24, v25\n"
    "v_add_f32 v25, 0x3727c5ac, v25\n"
    "v_cmp_u_f32 vcc, v20, v21\n"
    "v_cmp_u_f32 s[56:57], v25, v25\n"
    "s_or_b64 vcc, vcc, s[56:57]\n"
    "v_cvt_pk_bf16_f32 v22, v20, v21\n"
    "s_cbranch_scc1 L_Q_CHK\n"
    "L_Q_OK:\n"
    "ds_write_b32 v19, v22\n"
    "v_lshlrev_b32 v23, 16, v22\n"
    "v_and_b32 v24, 0xffff0000, v22\n"
    "v_sub_f32 v20, v20, v23\n"
    "v_sub_f32 v21, v21, v24\n"
    "v_cvt_pk_bf16_f32 v23, v20, v21\n"
    "ds_write_b32 v19, v23 offset:1152\n"
    "v_max_f32 v26, v30, v31\n"
    "v_max_f32 v25, 0x0da24260, v25\n"
    "v_rsq_f32 v25, v25\n"
    "v_max_f32_dpp v26, v26, v26 quad_perm:[1,0,3,2] row_mask:0xf bank_mask:0xf\n"
    "v_mul_f32 v1, 0x3db8aa3b, v25\n"
    "s_add_u32 m0, s47, 4\n"
    "v_max_f32_dpp v26, v26, v26 quad_perm:[2,3,0,1] row_mask:0xf bank_mask:0xf\n"
    "s_movrels_b32 s55, s80\n"
    "v_cvt_u32_f32 v26, v26\n"
    "s_nop 0\n"
    "v_readfirstlane_b32 s54, v26\n"
    "s_waitcnt lgkmcnt(0)\n"
    "s_barrier\n"
    "s_add_u32 s3, s54, s55\n"
    "s_bitcmp1_b32 s47, 0\n"
    "s_cbranch_scc1 L_QK_B\n"
    "ds_read_b128 v[16:19], v48 offset:0\n"
    "ds_read_b128 v[20:23], v48 offset:64\n"
    "ds_read_b128 v[24:27], v48 offset:128\n"
    "ds_read_b128 v[28:31], v48 offset:192\n"
    "ds_read_b128 v[32:35], v48 offset:256\n"
    "ds_read_b128 v[36:39], v48 offset:320\n"
    "ds_read_b128 v[40:43], v48 offset:384\n"
    "ds_read_b128 v[44:47], v48 offset:448\n"
    "s_cmp_eq_u32 s47, 2\n"
    "s_cselect_b64 vcc, -1, 0\n"
    "v_lshlrev_b32 v2, 4, v0\n"
    "v_add_u32 v2, s52, v2\n"
    "s_waitcnt lgkmcnt(7)\n"
    "v_mfma_f32_16x16x32_bf16 v[8:11], a[0:3], v[16:19], 0\n"
    "v_mfma_f32_16x16x32_bf16 v[12:15], a[128:131], v[16:19], 0\n"
    "ds_read_b128 v[16:19], v49 offset:0\n"
    "s_waitcnt lgkmcnt(7)\n"
    "v_mfma_f32_16x16x32_bf16 v[8:11], a[4:7], v[20:23], v[8:11]\n"
    "v_mfma_f32_16x16x32_bf16 v[12:15], a[132:135], v[20:23], v[12:15]\n"
    "ds_read_b128 v[20:23], v49 offset:64\n"
    "s_waitcnt lgkmcnt(7)\n"
    "v_mfma_f32_16x16x32_bf16 v[8:11], a[8:11], v[24:27], v[8:11]\n"
    "v_mfma_f32_16x16x32_bf16 v[12:15], a[136:139], v[24:27], v[12:15]\n"
    "ds_read_b128 v[24:27], v49 offset:128\n"
    "s_waitcnt lgkmcnt(7)\n"
    "v_mfma_f32_16x16x32_bf16 v[8:11], a[12:15], v[28:31], v[8:11]\n"
    "v_mfma_f32_16x16x32_bf16 v[12:15], a[140:143], v[28:31], v[12:15]\n"
    "ds_read_b128 v[28:31], v49 offset:192\n"
    "s_waitcnt lgkmcnt(7)\n"
    "v_mfma_f32_16x16x32_bf16 v[8:11], a[16:19], v[32:35], v[8:11]\n"
    "v_mfma_f32_16x16x32_bf16 v[12:15], a[144:147], v[32:35], v[12:15]\n"
    "ds_read_b128 v[32:35], v49 offset:256\n"
    "s_waitcnt lgkmcnt(7)\n"
    "v_mfma_f32_16x16x32_bf16 v[8:11], a[20:23], v[36:39], v[8:11]\n"
    "v_mfma_f32_16x16x32_bf16 v[12:15], a[148:151], v[36:39], v[12:15]\n"
    "ds_read_b128 v[36:39], v49 offset:320\n"
    "s_waitcnt lgkmcnt(7)\n"
    "v_mfma_f32_16x16x32_bf16 v[8:11], a[24:27], v[40:43], v[8:11]\n"
    "v_mfma_f32_16x16x32_bf16 v[12:15], a[152:155], v[40:43], v[12:15]\n"
    "ds_read_b128 v[40:43], v49 offset:384\n"
    "s_waitcnt lgkmcnt(7)\n"
    "v_mfma_f32_16x16x32_bf16 v[8:11], a[28:31], v[44:47], v[8:11]\n"
    "v_mfma_f32_16x16x32_bf16 v[12:15], a[156:159], v[44:47], v[12:15]\n"
    "ds_read_b128 v[44:47], v49 offset:448\n"
    "s_waitcnt lgkmcnt(7)\n"
    "v_mfma_f32_16x16x32_bf16 v[8:11], a[32:35], v[16:19], v[8:11]\n"
    "s_cbranch_vccz L_u2\n"
    "ds_read_b128 a[0:3], v2 offset:16384\n"
    "L_u2:\n"
    "v_mfma_f32_16x16x32_bf16 v[12:15], a[160:163], v[16:19], v[12:15]\n"
    "ds_read_b128 v[16:19], v50 offset:0\n"
    "s_waitcnt lgkmcnt(7)\n"
    "v_mfma_f32_16x16x32_bf16 v[8:11], a[36:39], v[20:23], v[8:11]\n"
    "s_cbranch_vccz L_u3\n"
    "ds_read_b128 a[4:7], v2 offset:17408\n"
    "L_u3:\n"
    "v_mfma_f32_16x16x32_bf16 v[12:15], a[164:167], v[20:23], v[12:15]\n"
    "ds_read_b128 v[20:23], v50 offset:64\n"
    "s_waitcnt lgkmcnt(7)\n"
    "v_mfma_f32_16x16x32_bf16 v[8:11], a[40:43], v[24:27], v[8:11]\n"
    "s_cbranch_vccz L_u4\n"
    "ds_read_b128 a[8:11], v2 offset:18432\n"
    "L_u4:\n"
    "v_mfma_f32_16x16x32_bf16 v[12:15], a[168:171], v[24:27], v[12:15]\n"
    "ds_read_b128 v[24:27], v50 offset:128\n"
    "s_waitcnt lgkmcnt(7)\n"
    "v_mfma_f32_16x16x32_bf16 v[8:11], a[44:47], v[28:31], v[8:11]\n"
    "s_cbranch_vccz L_u5\n"
    "ds_read_b128 a[12:15], v2 offset:19456\n"
    "L_u5:\n"
    "v_mfma_f32_16x16x32_bf16 v[12:15], a[172:175], v[28:31], v[12:15]\n"
    "ds_read_b128 v[28:31], v50 offset:192\n"
    "s_waitcnt lgkmcnt(7)\n"
    "v_mfma_f32_16x16x32_bf16 v[8:11], a[48:51], v[32:35], v[8:11]\n"
    "s_cbranch_vccz L_u6\n"
    "ds_read_b128 a[16:19], v2 offset:20480\n"
    "L_u6:\n"
    "v_mfma_f32_16x16x32_bf16 v[12:15], a[176:179], v[32:35], v[12:15]\n"
    "ds_read_b128 v[32:35], v50 offset:256\n"
    "s_waitcnt lgkmcnt(7)\n"
    "v_mfma_f32_16x16x32_bf16 v[8:11], a[52:55], v[36:39], v[8:11]\n"
    "s_cbranch_vccz L_u7\n"
    "ds_read_b128 a[20:23], v2 offset:21504\n"
    "L_u7:\n"
    "v_mfma_f32_16x16x32_bf16 v[12:15], a[180:183], v[36:39], v[12:15]\n"
    "ds_read_b128 v[36:39], v50 offset:320\n"
    "s_waitcnt lgkmcnt(7)\n"
    "v_mfma_f32_16x16x32_bf16 v[8:11], a[56:59], v[40:43], v[8:11]\n"
    "s_cbranch_vccz L_u8\n"
    "ds_read_b128 a[24:27], v2 offset:22528\n"
    "L_u8:\n"
    "v_mfma_f32_16x16x32_bf16 v[12:15], a[184:187], v[40:43], v[12:15]\n"
    "ds_read_b128 v[40:43], v50 offset:384\n"
    "s_waitcnt lgkmcnt(7)\n"
    "v_mfma_f32_16x16x32_bf16 v[8:11], a[60:63], v[44:47], v[8:11]\n"
    "s_cbranch_vccz L_u9\n"
    "ds_read_b128 a[28:31], v2 offset:23552\n"
    "L_u9:\n"
    "v_mfma_f32_16x16x32_bf16 v[12:15], a[188:191], v[44:47], v[12:15]\n"
    "ds_read_b128 v[44:47], v50 offset:448\n"
    "s_waitcnt lgkmcnt(7)\n"
    "v_mfma_f32_16x16x32_bf16 v[8:11], a[64:67], v[16:19], v[8:11]\n"
    "s_cbranch_vccz L_u10\n"
    "ds_read_b128 a[128:131], v2 offset:24576\n"
    "L_u10:\n"
    "v_mfma_f32_16x16x32_bf16 v[12:15], a[192:195], v[16:19], v[12:15]\n"
    "ds_read_b128 v[16:19], v51 offset:0\n"
    "s_waitcnt lgkmcnt(7)\n"
    "v_mfma_f32_16x16x32_bf16 v[8:11], a[68:71], v[20:23], v[8:11]\n"
    "s_cbranch_vccz L_u11\n"
    "ds_read_b128 a[132:135], v2 offset:25600\n"
    "L_u11:\n"
    "v_mfma_f32_16x16x32_bf16 v[12:15], a[196:199], v[20:23], v[12:15]\n"
    "ds_read_b128 v[20:23], v51 offset:64\n"
    "s_waitcnt lgkmcnt(7)\n"
    "v_mfma_f32_16x16x32_bf16 v[8:11], a[72:75], v[24:27], v[8:11]\n"
    "s_cbranch_vccz L_u12\n"
    "ds_read_b128 a[136:139], v2 offset:26624\n"
    "s_add_u32 m0, s52, 16384\n"
    "s_add_u32 s61, s1, 212992\n"
    "s_and_b32 s61, s61, 0x3ffff\n"
    "s_add_u32 s61, s61, s50\n"
    "s_add_u32 s63, s61, 0\n"
    "buffer_load_dwordx4 v63, s[12:15], s63 offen offset:0 nt lds\n"
    "L_u12:\n"
    "v_mfma_f32_16x16x32_bf16 v[12:15], a[200:203], v[24:27], v[12:15]\n"
    "ds_read_b128 v[24:27], v51 offset:128\n"
    "s_waitcnt lgkmcnt(7)\n"
    "v_mfma_f32_16x16x32_bf16 v[8:11], a[76:79], v[28:31], v[8:11]\n"
    "s_cbranch_vccz L_u13\n"
    "ds_read_b128 a[140:143], v2 offset:27648\n"
    "s_add_u32 s63, s61, 3072\n"
    "buffer_load_dwordx4 v63, s[12:15], s63 offen offset:1024 nt lds\n"
    "L_u13:\n"
    "v_mfma_f32_16x16x32_bf16 v[12:15], a[204:207], v[28:31], v[12:15]\n"
    "ds_read_b128 v[28:31], v51 offset:192\n"
    "s_waitcnt lgkmcnt(7)\n"
    "v_mfma_f32_16x16x32_bf16 v[8:11], a[80:83], v[32:35], v[8:11]\n"
    "s_cbranch_vccz L_u14\n"
    "s_add_u32 s63, s61, 6144\n"
    "buffer_load_dwordx4 v63, s[12:15], s63 offen offset:2048 nt lds\n"
    "L_u14:\n"
    "v_mfma_f32_16x16x32_bf16 v[12:15], a[208:211], v[32:35], v[12:15]\n"
    "ds_read_b128 v[32:35], v51 offset:256\n"
    "s_waitcnt lgkmcnt(7)\n"
    "v_mfma_f32_16x16x32_bf16 v[8:11], a[84:87], v[36:39], v[8:11]\n"
    "s_cbranch_vccz L_u15\n"
    "s_add_u32 s63, s61, 9216\n"
    "buffer_load_dwordx4 v63, s[12:15], s63 offen offset:3072 nt lds\n"
    "L_u15:\n"
    "v_mfma_f32_16x16x32_bf16 v[12:15], a[212:215], v[36:39], v[12:15]\n"
    "ds_read_b128 v[36:39], v51 offset:320\n"
    "s_waitcnt lgkmcnt(7)\n"
    "v_mfma_f32_16x16x32_bf16 v[8:11], a[88:91], v[40:43], v[8:11]\n"
    "s_cbranch_vccz L_u16\n"
    "s_add_u32 m0, s52, 20480\n"
    "s_add_u32 s61, s1, 229376\n"
    "s_and_b32 s61, s61, 0x3ffff\n"
    "s_add_u32 s61, s61, s50\n"
    "s_add_u32 s63, s61, 0\n"
    "buffer_load_dwordx4 v63, s[12:15], s63 offen offset:0 nt lds\n"
    "L_u16:\n"
    "v_mfma_f32_16x16x32_bf16 v[12:15], a[216:219], v[40:43], v[12:15]\n"
    "ds_read_b128 v[40:43], v51 offset:384\n"
    "s_waitcnt lgkmcnt(7)\n"
    "v_mfma_f32_16x16x32_bf16 v[8:11], a[92:95], v[44:47], v[8:11]\n"
    "s_cbranch_vccz L_u17\n"
    "s_add_u32 s63, s61, 3072\n"
    "buffer_load_dwordx4 v63, s[12:15], s63 offen offset:1024 nt lds\n"
    "L_u17:\n"
    "v_mfma_f32_16x16x32_bf16 v[12:15], a[220:223], v[44:47], v[12:15]\n"
    "ds_read_b128 v[44:47], v51 offset:448\n"
    "s_waitcnt lgkmcnt(7)\n"
    "v_mfma_f32_16x16x32_bf16 v[8:11], a[96:99], v[16:19], v[8:11]\n"
    "s_cbranch_vccz L_u18\n"
    "s_add_u32 s63, s61, 6144\n"
    "buffer_load_dwordx4 v63, s[12:15], s63 offen offset:2048 nt lds\n"
    "L_u18:\n"
    "v_mfma_f32_16x16x32_bf16 v[12:15], a[224:227], v[16:19], v[12:15]\n"
    "s_waitcnt lgkmcnt(6)\n"
    "v_mfma_f32_16x16x32_bf16 v[8:11], a[100:103], v[20:23], v[8:11]\n"
    "s_cbranch_vccz L_u19\n"
    "s_add_u32 s63, s61, 9216\n"
    "buffer_load_dwordx4 v63, s[12:15], s63 offen offset:3072 nt lds\n"
    "L_u19:\n"
    "v_mfma_f32_16x16x32_bf16 v[12:15], a[228:231], v[20:23], v[12:15]\n"
    "s_waitcnt lgkmcnt(5)\n"
    "v_mfma_f32_16x16x32_bf16 v[8:11], a[104:107], v[24:27], v[8:11]\n"
    "s_cbranch_vccz L_u20\n"
    "s_add_u32 m0, s52, 24576\n"
    "s_add_u32 s61, s1, 245760\n"
    "s_and_b32 s61, s61, 0x3ffff\n"
    "s_add_u32 s61, s61, s50\n"
    "s_add_u32 s63, s61, 0\n"
    "buffer_load_dwordx4 v63, s[12:15], s63 offen offset:0 nt lds\n"
    "L_u20:\n"
    "v_mfma_f32_16x16x32_bf16 v[12:15], a[232:235], v[24:27], v[12:15]\n"
    "s_waitcnt lgkmcnt(4)\n"
    "v_mfma_f32_16x16x32_bf16 v[8:11], a[108:111], v[28:31], v[8:11]\n"
    "s_cbranch_vccz L_u21\n"
    "s_add_u32 s63, s61, 3072\n"
    "buffer_load_dwordx4 v63, s[12:15], s63 offen offset:1024 nt lds\n"
    "L_u21:\n"
    "v_mfma_f32_16x16x32_bf16 v[12:15], a[236:239], v[28:31], v[12:15]\n"
    "s_waitcnt lgkmcnt(3)\n"
    "v_mfma_f32_16x16x32_bf16 v[8:11], a[112:115], v[32:35], v[8:11]\n"
    "s_cbranch_vccz L_u22\n"
    "s_add_u32 s63, s61, 6144\n"
    "buffer_load_dwordx4 v63, s[12:15], s63 offen offset:2048 nt lds\n"
    "L_u22:\n"
    "v_mfma_f32_16x16x32_bf16 v[12:15], a[240:243], v[32:35], v[12:15]\n"
    "s_waitcnt lgkmcnt(2)\n"
    "v_mfma_f32_16x16x32_bf16 v[8:11], a[116:119], v[36:39], v[8:11]\n"
    "s_cbranch_vccz L_u23\n"
    "s_add_u32 s63, s61, 9216\n"
    "buffer_load_dwordx4 v63, s[12:15], s63 offen offset:3072 nt lds\n"
    "L_u23:\n"
    "v_mfma_f32_16x16x32_bf16 v[12:15], a[244:247], v[36:39], v[12:15]\n"
    "s_waitcnt lgkmcnt(1)\n"
    "v_mfma_f32_16x16x32_bf16 v[8:11], a[120:123], v[40:43], v[8:11]\n"
    "v_mfma_f32_16x16x32_bf16 v[12:15], a[248:251], v[40:43], v[12:15]\n"
    "s_waitcnt lgkmcnt(0)\n"
    "v_mfma_f32_16x16x32_bf16 v[8:11], a[124:127], v[44:47], v[8:11]\n"
    "v_mfma_f32_16x16x32_bf16 v[12:15], a[252:255], v[44:47], v[12:15]\n"
    "s_nop 6\n"
    "v_add_f32_dpp v8, v8, v8 quad_perm:[2,3,0,1] row_mask:0xf bank_mask:0xf\n"
    "v_add_f32_dpp v9, v9, v9 quad_perm:[2,3,0,1] row_mask:0xf bank_mask:0xf\n"
    "v_add_f32_dpp v10, v10, v10 quad_perm:[2,3,0,1] row_mask:0xf bank_mask:0xf\n"
    "v_add_f32_dpp v11, v11, v11 quad_perm:[2,3,0,1] row_mask:0xf bank_mask:0xf\n"
    "v_add_f32_dpp v12, v12, v12 quad_perm:[2,3,0,1] row_mask:0xf bank_mask:0xf\n"
    "v_add_f32_dpp v13, v13, v13 quad_perm:[2,3,0,1] row_mask:0xf bank_mask:0xf\n"
    "v_add_f32_dpp v14, v14, v14 quad_perm:[2,3,0,1] row_mask:0xf bank_mask:0xf\n"
    "v_add_f32_dpp v15, v15, v15 quad_perm:[2,3,0,1] row_mask:0xf bank_mask:0xf\n"
    "v_max3_f32 v16, v8, v9, v10\n"
    "v_max3_f32 v17, v11, v12, v13\n"
    "v_max3_f32 v16, v16, v14, v15\n"
    "v_max_f32 v16, v16, v17\n"
    "s_nop 1\n"
    "v_max_f32_dpp v16, v16, v16 row_ror:4 row_mask:0xf bank_mask:0xf\n"
    "s_nop 1\n"
    "v_max_f32_dpp v16, v16, v16 row_ror:8 row_mask:0xf bank_mask:0xf\n"
    "v_mov_b32 v17, v16\n"
    "s_nop 1\n"
    "v_permlane16_swap_b32 v16, v17\n"
    "v_max_f32 v16, v16, v17\n"
    "v_mov_b32 v17, v16\n"
    "s_nop 1\n"
    "v_permlane32_swap_b32 v16, v17\n"
    "v_max_f32 v16, v16, v17\n"
    "v_mul_f32 v18, v16, v1\n"
    "v_fma_f32 v8, v8, v1, -v18\n"
    "v_fma_f32 v9, v9, v1, -v18\n"
    "v_fma_f32 v10, v10, v1, -v18\n"
    "v_fma_f32 v11, v11, v1, -v18\n"
    "v_fma_f32 v12, v12, v1, -v18\n"
    "v_fma_f32 v13, v13, v1, -v18\n"
    "v_fma_f32 v14, v14, v1, -v18\n"
    "v_fma_f32 v15, v15, v1, -v18\n"
    "v_exp_f32 v8, v8\n"
    "v_exp_f32 v9, v9\n"
    "v_exp_f32 v10, v10\n"
    "v_exp_f32 v11, v11\n"
    "v_exp_f32 v12, v12\n"
    "v_exp_f32 v13, v13\n"
    "v_exp_f32 v14, v14\n"
    "v_exp_f32 v15, v15\n"
    "v_and_b32 v20, v61, v8\n"
    "v_and_b32 v21, v61, v9\n"
    "v_and_b32 v22, v61, v10\n"
    "v_and_b32 v23, v61, v11\n"
    "v_and_b32 v24, v61, v12\n"
    "v_and_b32 v25, v61, v13\n"
    "v_and_b32 v26, v61, v14\n"
    "v_and_b32 v27, v61, v15\n"
    "v_pk_add_f32 v[28:29], v[8:9], v[10:11]\n"
    "v_pk_add_f32 v[30:31], v[12:13], v[14:15]\n"
    "v_sub_f32 v20, v8, v20\n"
    "v_sub_f32 v21, v9, v21\n"
    "v_sub_f32 v22, v10, v22\n"
    "v_sub_f32 v23, v11, v23\n"
    "v_sub_f32 v24, v12, v24\n"
    "v_sub_f32 v25, v13, v25\n"
    "v_sub_f32 v26, v14, v26\n"
    "v_sub_f32 v27, v15, v27\n"
    "v_pk_add_f32 v[28:29], v[28:29], v[30:31]\n"
    "v_perm_b32 v8, v21, v20, s66\n"
    "v_perm_b32 v9, v23, v22, s66\n"
    "v_perm_b32 v10, v25, v24, s66\n"
    "v_perm_b32 v11, v27, v26, s66\n"
    "v_add_f32 v19, v28, v29\n"
    "ds_write_b64 v60, v[8:9]\n"
    "ds_write_b64 v60, v[10:11] offset:512\n"
    "v_add_f32_dpp v19, v19, v19 row_ror:4 row_mask:0xf bank_mask:0xf\n"
    "s_nop 1\n"
    "v_add_f32_dpp v19, v19, v19 row_ror:8 row_mask:0xf bank_mask:0xf\n"
    "v_mov_b32 v17, v19\n"
    "s_nop 1\n"
    "v_permlane16_swap_b32 v19, v17\n"
    "v_add_f32 v19, v19, v17\n"
    "v_mov_b32 v17, v19\n"
    "s_nop 1\n"
    "v_permlane32_swap_b32 v19, v17\n"
    "v_add_f32 v19, v19, v17\n"
    "ds_write_b64 v62, v[18:19]\n"
    "s_waitcnt lgkmcnt(0)\n"
    "s_barrier\n"
    "s_cmp_eq_u32 s47, 0\n"
    "s_cbranch_scc1 L_VMID\n"
    "s_waitcnt vmcnt(0)\n"
    "L_PV_A:\n"
    "ds_read_b128 v[24:27], v52 offset:0\n"
    "ds_read_b128 v[28:31], v52 offset:256\n"
    "v_bfe_u32 v61, v0, 2, 2\n"
    "v_lshlrev_b32 v61, 4, v61\n"
    "v_and_b32 v60, 1, v0\n"
    "v_lshl_or_b32 v61, v60, 3, v61\n"
    "ds_read_b64 v[62:63], v61 offset:3328\n"
    "s_waitcnt lgkmcnt(2)\n"
    "v_mfma_f32_16x16x32_bf16 v[8:11], v[64:67], v[24:27], 0\n"
    "ds_read_b128 v[32:35], v55 offset:256\n"
    "ds_read_b64_tr_b16 v[36:37], v56 offset:16384\n"
    "ds_read_b64_tr_b16 v[38:39], v56 offset:18432\n"
    "ds_read_b64_tr_b16 v[40:41], v57 offset:16384\n"
    "ds_read_b64_tr_b16 v[42:43], v57 offset:18432\n"
    "ds_read_b64_tr_b16 v[44:45], v58 offset:16384\n"
    "ds_read_b64_tr_b16 v[46:47], v58 offset:18432\n"
    "ds_read_b64_tr_b16 v[48:49], v59 offset:16384\n"
    "ds_read_b64_tr_b16 v[50:51], v59 offset:18432\n"
    "v_mfma_f32_16x16x32_bf16 v[12:15], v[68:71], v[24:27], 0\n"
    "v_mfma_f32_16x16x32_bf16 v[16:19], v[72:75], v[24:27], 0\n"
    "v_mfma_f32_16x16x32_bf16 v[20:23], v[76:79], v[24:27], 0\n"
    "s_waitcnt lgkmcnt(10)\n"
    "v_mfma_f32_16x16x32_bf16 v[8:11], v[80:83], v[28:31], v[8:11]\n"
    "ds_read_b128 v[24:27], v52 offset:512\n"
    "s_waitcnt lgkmcnt(10)\n"
    "v_max_f32_dpp v61, v62, v62 row_ror:4 row_mask:0xf bank_mask:0xf\n"
    "v_mfma_f32_16x16x32_bf16 v[12:15], v[84:87], v[28:31], v[12:15]\n"
    "s_nop 0\n"
    "v_max_f32_dpp v61, v61, v61 row_ror:8 row_mask:0xf bank_mask:0xf\n"
    "v_mfma_f32_16x16x32_bf16 v[16:19], v[88:91], v[28:31], v[16:19]\n"
    "v_sub_f32 v60, v62, v61\n"
    "v_mfma_f32_16x16x32_bf16 v[20:23], v[92:95], v[28:31], v[20:23]\n"
    "v_exp_f32 v60, v60\n"
    "s_waitcnt lgkmcnt(9)\n"
    "s_waitcnt lgkmcnt(1)\n"
    "v_mfma_f32_16x16x32_bf16 v[8:11], v[36:39], v[32:35], v[8:11]\n"
    "ds_read_b128 v[28:31], v52 offset:768\n"
    "v_mov_b32 v62, v61\n"
    "v_mfma_f32_16x16x32_bf16 v[12:15], v[40:43], v[32:35], v[12:15]\n"
    "v_mul_f32 v63, v63, v60\n"
    "v_mfma_f32_16x16x32_bf16 v[16:19], v[44:47], v[32:35], v[16:19]\n"
    "v_lshlrev_b32 v61, 8, v0\n"
    "v_mfma_f32_16x16x32_bf16 v[20:23], v[48:51], v[32:35], v[20:23]\n"
    "v_add_f32_dpp v63, v63, v63 row_ror:4 row_mask:0xf bank_mask:0xf\n"
    "s_waitcnt lgkmcnt(1)\n"
    "v_mfma_f32_16x16x32_bf16 v[8:11], v[96:99], v[24:27], v[8:11]\n"
    "ds_read_b128 v[32:35], v55 offset:512\n"
    "ds_read_b64_tr_b16 v[36:37], v56 offset:20480\n"
    "ds_read_b64_tr_b16 v[38:39], v56 offset:22528\n"
    "ds_read_b64_tr_b16 v[40:41], v57 offset:20480\n"
    "ds_read_b64_tr_b16 v[42:43], v57 offset:22528\n"
    "ds_read_b64_tr_b16 v[44:45], v58 offset:20480\n"
    "ds_read_b64_tr_b16 v[46:47], v58 offset:22528\n"
    "ds_read_b64_tr_b16 v[48:49], v59 offset:20480\n"
    "ds_read_b64_tr_b16 v[50:51], v59 offset:22528\n"
    "v_min_u32 v62, -2, v62\n"
    "v_mfma_f32_16x16x32_bf16 v[12:15], v[100:103], v[24:27], v[12:15]\n"
    "v_add_f32_dpp v63, v63, v63 row_ror:8 row_mask:0xf bank_mask:0xf\n"
    "v_mfma_f32_16x16x32_bf16 v[16:19], v[104:107], v[24:27], v[16:19]\n"
    "v_min_u32 v63, -2, v63\n"
    "s_lshl_b32 s54, s45, 2\n"
    "s_add_u32 s54, s54, s44\n"
    "s_lshl_b32 s54, s54, 9\n"
    "s_and_b32 s55, s46, 7\n"
    "s_lshl_b32 s55, s55, 5\n"
    "s_add_u32 s54, s54, s55\n"
    "s_and_b32 s55, s46, 24\n"
    "s_add_u32 s54, s54, s55\n"
    "s_add_u32 s54, s54, s53\n"
    "s_add_u32 s54, s54, 524288\n"
    "s_mov_b64 exec, 3\n"
    "buffer_store_dwordx2 v[62:63], v61, s[4:7], s54 offen sc1\n"
    "s_mov_b64 exec, -1\n"
    "v_mfma_f32_16x16x32_bf16 v[20:23], v[108:111], v[24:27], v[20:23]\n"
    "s_waitcnt lgkmcnt(9)\n"
    "v_mfma_f32_16x16x32_bf16 v[8:11], v[112:115], v[28:31], v[8:11]\n"
    "ds_read_b128 v[24:27], v53 offset:0\n"
    "v_mfma_f32_16x16x32_bf16 v[12:15], v[116:119], v[28:31], v[12:15]\n"
    "v_mfma_f32_16x16x32_bf16 v[16:19], v[120:123], v[28:31], v[16:19]\n"
    "v_mfma_f32_16x16x32_bf16 v[20:23], v[124:127], v[28:31], v[20:23]\n"
    "s_waitcnt lgkmcnt(9)\n"
    "s_waitcnt lgkmcnt(1)\n"
    "v_mfma_f32_16x16x32_bf16 v[8:11], v[36:39], v[32:35], v[8:11]\n"
    "ds_read_b128 v[28:31], v53 offset:256\n"
    "v_mfma_f32_16x16x32_bf16 v[12:15], v[40:43], v[32:35], v[12:15]\n"
    "v_mfma_f32_16x16x32_bf16 v[16:19], v[44:47], v[32:35], v[16:19]\n"
    "v_mfma_f32_16x16x32_bf16 v[20:23], v[48:51], v[32:35], v[20:23]\n"
    "s_waitcnt lgkmcnt(1)\n"
    "v_mfma_f32_16x16x32_bf16 v[8:11], v[128:131], v[24:27], v[8:11]\n"
    "ds_read_b128 v[32:35], v55 offset:768\n"
    "ds_read_b64_tr_b16 v[36:37], v56 offset:24576\n"
    "ds_read_b64_tr_b16 v[38:39], v56 offset:26624\n"
    "ds_read_b64_tr_b16 v[40:41], v57 offset:24576\n"
    "ds_read_b64_tr_b16 v[42:43], v57 offset:26624\n"
    "ds_read_b64_tr_b16 v[44:45], v58 offset:24576\n"
    "ds_read_b64_tr_b16 v[46:47], v58 offset:26624\n"
    "ds_read_b64_tr_b16 v[48:49], v59 offset:24576\n"
    "ds_read_b64_tr_b16 v[50:51], v59 offset:26624\n"
    "v_mfma_f32_16x16x32_bf16 v[12:15], v[132:135], v[24:27], v[12:15]\n"
    "v_mfma_f32_16x16x32_bf16 v[16:19], v[136:139], v[24:27], v[16:19]\n"
    "v_mfma_f32_16x16x32_bf16 v[20:23], v[140:143], v[24:27], v[20:23]\n"
    "s_waitcnt lgkmcnt(9)\n"
    "v_mfma_f32_16x16x32_bf16 v[8:11], v[144:147], v[28:31], v[8:11]\n"
    "ds_read_b128 v[24:27], v53 offset:512\n"
    "v_mfma_f32_16x16x32_bf16 v[12:15], v[148:151], v[28:31], v[12:15]\n"
    "v_mfma_f32_16x16x32_bf16 v[16:19], v[152:155], v[28:31], v[16:19]\n"
    "v_mfma_f32_16x16x32_bf16 v[20:23], v[156:159], v[28:31], v[20:23]\n"
    "s_waitcnt lgkmcnt(9)\n"
    "s_waitcnt lgkmcnt(1)\n"
    "v_mfma_f32_16x16x32_bf16 v[8:11], v[36:39], v[32:35], v[8:11]\n"
    "ds_read_b128 v[28:31], v53 offset:768\n"
    "v_mfma_f32_16x16x32_bf16 v[12:15], v[40:43], v[32:35], v[12:15]\n"
    "v_mfma_f32_16x16x32_bf16 v[16:19], v[44:47], v[32:35], v[16:19]\n"
    "v_mfma_f32_16x16x32_bf16 v[20:23], v[48:51], v[32:35], v[20:23]\n"
    "s_cmp_eq_u32 s47, 0\n"
    "s_cbranch_scc1 L_VREST\n"
    "L_PV_A2:\n"
    "s_waitcnt lgkmcnt(1)\n"
    "v_mfma_f32_16x16x32_bf16 v[8:11], v[160:163], v[24:27], v[8:11]\n"
    "ds_read_b128 v[32:35], v54 offset:0\n"
    "ds_read_b64_tr_b16 v[36:37], v56 offset:32768\n"
    "ds_read_b64_tr_b16 v[38:39], v56 offset:34816\n"
    "ds_read_b64_tr_b16 v[40:41], v57 offset:32768\n"
    "ds_read_b64_tr_b16 v[42:43], v57 offset:34816\n"
    "ds_read_b64_tr_b16 v[44:45], v58 offset:32768\n"
    "ds_read_b64_tr_b16 v[46:47], v58 offset:34816\n"
    "ds_read_b64_tr_b16 v[48:49], v59 offset:32768\n"
    "ds_read_b64_tr_b16 v[50:51], v59 offset:34816\n"
    "v_mfma_f32_16x16x32_bf16 v[12:15], v[164:167], v[24:27], v[12:15]\n"
    "v_mfma_f32_16x16x32_bf16 v[16:19], v[168:171], v[24:27], v[16:19]\n"
    "v_mfma_f32_16x16x32_bf16 v[20:23], v[172:175], v[24:27], v[20:23]\n"
    "s_waitcnt lgkmcnt(9)\n"
    "v_mfma_f32_16x16x32_bf16 v[8:11], v[176:179], v[28:31], v[8:11]\n"
    "ds_read_b128 v[24:27], v55 offset:0\n"
    "v_mfma_f32_16x16x32_bf16 v[12:15], v[180:183], v[28:31], v[12:15]\n"
    "v_mfma_f32_16x16x32_bf16 v[16:19], v[184:187], v[28:31], v[16:19]\n"
    "v_mfma_f32_16x16x32_bf16 v[20:23], v[188:191], v[28:31], v[20:23]\n"
    "s_waitcnt lgkmcnt(9)\n"
    "v_mfma_f32_16x16x32_bf16 v[8:11], v[192:195], v[32:35], v[8:11]\n"
    "ds_read_b128 v[28:31], v54 offset:256\n"
    "v_mfma_f32_16x16x32_bf16 v[12:15], v[196:199], v[32:35], v[12:15]\n"
    "v_mfma_f32_16x16x32_bf16 v[16:19], v[200:203], v[32:35], v[16:19]\n"
    "v_mfma_f32_16x16x32_bf16 v[20:23], v[204:207], v[32:35], v[20:23]\n"
    "s_waitcnt lgkmcnt(1)\n"
    "s_waitcnt lgkmcnt(2)\n"
    "v_mfma_f32_16x16x32_bf16 v[8:11], v[36:39], v[24:27], v[8:11]\n"
    "ds_read_b128 v[32:35], v54 offset:512\n"
    "v_mfma_f32_16x16x32_bf16 v[12:15], v[40:43], v[24:27], v[12:15]\n"
    "v_mfma_f32_16x16x32_bf16 v[16:19], v[44:47], v[24:27], v[16:19]\n"
    "v_mfma_f32_16x16x32_bf16 v[20:23], v[48:51], v[24:27], v[20:23]\n"
    "s_waitcnt lgkmcnt(1)\n"
    "v_mfma_f32_16x16x32_bf16 v[8:11], v[208:211], v[28:31], v[8:11]\n"
    "ds_read_b128 v[24:27], v54 offset:768\n"
    "v_mfma_f32_16x16x32_bf16 v[12:15], v[212:215], v[28:31], v[12:15]\n"
    "v_mfma_f32_16x16x32_bf16 v[16:19], v[216:219], v[28:31], v[16:19]\n"
    "v_mfma_f32_16x16x32_bf16 v[20:23], v[220:223], v[28:31], v[20:23]\n"
    "s_waitcnt lgkmcnt(1)\n"
    "v_mfma_f32_16x16x32_bf16 v[8:11], v[224:227], v[32:35], v[8:11]\n"
    "v_mfma_f32_16x16x32_bf16 v[12:15], v[228:231], v[32:35], v[12:15]\n"
    "v_mfma_f32_16x16x32_bf16 v[16:19], v[232:235], v[32:35], v[16:19]\n"
    "v_mfma_f32_16x16x32_bf16 v[20:23], v[236:239], v[32:35], v[20:23]\n"
    "s_waitcnt lgkmcnt(0)\n"
    "v_mfma_f32_16x16x32_bf16 v[8:11], v[240:243], v[24:27], v[8:11]\n"
    "v_mfma_f32_16x16x32_bf16 v[12:15], v[244:247], v[24:27], v[12:15]\n"
    "v_mfma_f32_16x16x32_bf16 v[16:19], v[248:251], v[24:27], v[16:19]\n"
    "v_mfma_f32_16x16x32_bf16 v[20:23], v[252:255], v[24:27], v[20:23]\n"
    "s_branch L_FIN\n"
    "L_QK_B:\n"
    "s_nop 5\n"
    "ds_read_b128 v[16:19], v63 offset:0\n"
    "ds_read_b128 v[24:27], v48 offset:0\n"
    "ds_read_b128 v[28:31], v48 offset:8192\n"
    "ds_read_b128 v[20:23], v63 offset:64\n"
    "ds_read_b128 v[32:35], v49 offset:0\n"
    "ds_read_b128 v[36:39], v49 offset:8192\n"
    "ds_read_b128 v[40:43], v50 offset:0\n"
    "ds_read_b128 v[44:47], v50 offset:8192\n"
    "s_waitcnt lgkmcnt(7)\n"
    "s_waitcnt lgkmcnt(5)\n"
    "v_mfma_f32_16x16x32_bf16 v[8:11], v[24:27], v[16:19], 0\n"
    "v_mfma_f32_16x16x32_bf16 v[12:15], v[28:31], v[16:19], 0\n"
    "ds_read_b128 v[16:19], v63 offset:128\n"
    "ds_read_b128 v[24:27], v51 offset:0\n"
    "ds_read_b128 v[28:31], v51 offset:8192\n"
    "s_waitcnt lgkmcnt(7)\n"
    "s_waitcnt lgkmcnt(5)\n"
    "v_mfma_f32_16x16x32_bf16 v[8:11], v[32:35], v[20:23], v[8:11]\n"
    "v_mfma_f32_16x16x32_bf16 v[12:15], v[36:39], v[20:23], v[12:15]\n"
    "ds_read_b128 v[20:23], v63 offset:192\n"
    "ds_read_b128 v[32:35], v48 offset:256\n"
    "ds_read_b128 v[36:39], v48 offset:8448\n"
    "s_waitcnt lgkmcnt(5)\n"
    "s_waitcnt lgkmcnt(6)\n"
    "v_mfma_f32_16x16x32_bf16 v[8:11], v[40:43], v[16:19], v[8:11]\n"
    "v_mfma_f32_16x16x32_bf16 v[12:15], v[44:47], v[16:19], v[12:15]\n"
    "ds_read_b128 v[16:19], v63 offset:256\n"
    "ds_read_b128 v[40:43], v49 offset:256\n"
    "ds_read_b128 v[44:47], v49 offset:8448\n"
    "s_waitcnt lgkmcnt(5)\n"
    "s_waitcnt lgkmcnt(6)\n"
    "v_mfma_f32_16x16x32_bf16 v[8:11], v[24:27], v[20:23], v[8:11]\n"
    "v_mfma_f32_16x16x32_bf16 v[12:15], v[28:31], v[20:23], v[12:15]\n"
    "ds_read_b128 v[20:23], v63 offset:320\n"
    "ds_read_b128 v[24:27], v50 offset:256\n"
    "ds_read_b128 v[28:31], v50 offset:8448\n"
    "s_waitcnt lgkmcnt(5)\n"
    "s_waitcnt lgkmcnt(6)\n"
    "v_mfma_f32_16x16x32_bf16 v[8:11], v[32:35], v[16:19], v[8:11]\n"
    "v_mfma_f32_16x16x32_bf16 v[12:15], v[36:39], v[16:19], v[12:15]\n"
    "ds_read_b128 v[16:19], v63 offset:384\n"
    "ds_read_b128 v[32:35], v51 offset:256\n"
    "ds_read_b128 v[36:39], v51 offset:8448\n"
    "s_waitcnt lgkmcnt(5)\n"
    "s_waitcnt lgkmcnt(6)\n"
    "v_mfma_f32_16x16x32_bf16 v[8:11], v[40:43], v[20:23], v[8:11]\n"
    "v_mfma_f32_16x16x32_bf16 v[12:15], v[44:47], v[20:23], v[12:15]\n"
    "ds_read_b128 v[20:23], v63 offset:448\n"
    "s_waitcnt lgkmcnt(3)\n"
    "s_waitcnt lgkmcnt(4)\n"
    "v_mfma_f32_16x16x32_bf16 v[8:11], v[24:27], v[16:19], v[8:11]\n"
    "v_mfma_f32_16x16x32_bf16 v[12:15], v[28:31], v[16:19], v[12:15]\n"
    "s_waitcnt lgkmcnt(0)\n"
    "s_waitcnt lgkmcnt(1)\n"
    "v_mfma_f32_16x16x32_bf16 v[8:11], v[32:35], v[20:23], v[8:11]\n"
    "v_mfma_f32_16x16x32_bf16 v[12:15], v[36:39], v[20:23], v[12:15]\n"
    "s_nop 6\n"
    "v_add_f32_dpp v8, v8, v8 quad_perm:[2,3,0,1] row_mask:0xf bank_mask:0xf\n"
    "v_add_f32_dpp v9, v9, v9 quad_perm:[2,3,0,1] row_mask:0xf bank_mask:0xf\n"
    "v_add_f32_dpp v10, v10, v10 quad_perm:[2,3,0,1] row_mask:0xf bank_mask:0xf\n"
    "v_add_f32_dpp v11, v11, v11 quad_perm:[2,3,0,1] row_mask:0xf bank_mask:0xf\n"
    "v_add_f32_dpp v12, v12, v12 quad_perm:[2,3,0,1] row_mask:0xf bank_mask:0xf\n"
    "v_add_f32_dpp v13, v13, v13 quad_perm:[2,3,0,1] row_mask:0xf bank_mask:0xf\n"
    "v_add_f32_dpp v14, v14, v14 quad_perm:[2,3,0,1] row_mask:0xf bank_mask:0xf\n"
    "v_add_f32_dpp v15, v15, v15 quad_perm:[2,3,0,1] row_mask:0xf bank_mask:0xf\n"
    "v_max3_f32 v16, v8, v9, v10\n"
    "v_max3_f32 v17, v11, v12, v13\n"
    "v_max3_f32 v16, v16, v14, v15\n"
    "v_max_f32 v16, v16, v17\n"
    "v_mov_b32 v17, v16\n"
    "s_nop 1\n"
    "v_permlane16_swap_b32 v16, v17\n"
    "v_max_f32 v16, v16, v17\n"
    "v_mov_b32 v17, v16\n"
    "s_nop 1\n"
    "v_permlane32_swap_b32 v16, v17\n"
    "v_max_f32 v16, v16, v17\n"
    "v_mul_f32 v18, v16, v1\n"
    "v_fma_f32 v8, v8, v1, -v18\n"
    "v_fma_f32 v9, v9, v1, -v18\n"
    "v_fma_f32 v10, v10, v1, -v18\n"
    "v_fma_f32 v11, v11, v1, -v18\n"
    "v_fma_f32 v12, v12, v1, -v18\n"
    "v_fma_f32 v13, v13, v1, -v18\n"
    "v_fma_f32 v14, v14, v1, -v18\n"
    "v_fma_f32 v15, v15, v1, -v18\n"
    "v_exp_f32 v8, v8\n"
    "v_exp_f32 v9, v9\n"
    "v_exp_f32 v10, v10\n"
    "v_exp_f32 v11, v11\n"
    "v_exp_f32 v12, v12\n"
    "v_exp_f32 v13, v13\n"
    "v_exp_f32 v14, v14\n"
    "v_exp_f32 v15, v15\n"
    "v_and_b32 v20, v61, v8\n"
    "v_and_b32 v21, v61, v9\n"
    "v_and_b32 v22, v61, v10\n"
    "v_and_b32 v23, v61, v11\n"
    "v_and_b32 v24, v61, v12\n"
    "v_and_b32 v25, v61, v13\n"
    "v_and_b32 v26, v61, v14\n"
    "v_and_b32 v27, v61, v15\n"
    "v_pk_add_f32 v[28:29], v[8:9], v[10:11]\n"
    "v_pk_add_f32 v[30:31], v[12:13], v[14:15]\n"
    "v_sub_f32 v20, v8, v20\n"
    "v_sub_f32 v21, v9, v21\n"
    "v_sub_f32 v22, v10, v22\n"
    "v_sub_f32 v23, v11, v23\n"
    "v_sub_f32 v24, v12, v24\n"
    "v_sub_f32 v25, v13, v25\n"
    "v_sub_f32 v26, v14, v26\n"
    "v_sub_f32 v27, v15, v27\n"
    "v_pk_add_f32 v[28:29], v[28:29], v[30:31]\n"
    "v_perm_b32 v8, v21, v20, s66\n"
    "v_perm_b32 v9, v23, v22, s66\n"
    "v_perm_b32 v10, v25, v24, s66\n"
    "v_perm_b32 v11, v27, v26, s66\n"
    "v_add_f32 v19, v28, v29\n"
    "ds_write_b128 v60, v[8:11]\n"
    "v_mov_b32 v17, v19\n"
    "s_nop 1\n"
    "v_permlane16_swap_b32 v19, v17\n"
    "v_add_f32 v19, v19, v17\n"
    "v_mov_b32 v17, v19\n"
    "s_nop 1\n"
    "v_permlane32_swap_b32 v19, v17\n"
    "v_add_f32 v19, v19, v17\n"
    "ds_write_b64 v62, v[18:19]\n"
    "s_waitcnt lgkmcnt(0)\n"
    "s_barrier\n"
    "s_waitcnt vmcnt(0)\n"
    "ds_read_b128 v[24:27], v52 offset:0\n"
    "ds_read_b64_tr_b16 v[36:37], v56 offset:16384\n"
    "ds_read_b64_tr_b16 v[38:39], v56 offset:18432\n"
    "ds_read_b64_tr_b16 v[40:41], v57 offset:16384\n"
    "ds_read_b64_tr_b16 v[42:43], v57 offset:18432\n"
    "ds_read_b64_tr_b16 v[44:45], v58 offset:16384\n"
    "ds_read_b64_tr_b16 v[46:47], v58 offset:18432\n"
    "ds_read_b64_tr_b16 v[48:49], v59 offset:16384\n"
    "ds_read_b64_tr_b16 v[50:51], v59 offset:18432\n"
    "ds_read_b128 v[28:31], v53 offset:0\n"
    "v_bfe_u32 v61, v0, 2, 2\n"
    "v_lshlrev_b32 v61, 4, v61\n"
    "v_and_b32 v60, 1, v0\n"
    "v_lshl_or_b32 v61, v60, 3, v61\n"
    "ds_read_b64 v[62:63], v61 offset:3328\n"
    "s_waitcnt lgkmcnt(10)\n"
    "s_waitcnt lgkmcnt(6)\n"
    "v_mfma_f32_16x16x32_bf16 v[8:11], v[36:39], v[24:27], 0\n"
    "v_mfma_f32_16x16x32_bf16 v[12:15], v[40:43], v[24:27], 0\n"
    "ds_read_b64_tr_b16 v[36:37], v56 offset:20480\n"
    "ds_read_b64_tr_b16 v[38:39], v56 offset:22528\n"
    "ds_read_b64_tr_b16 v[40:41], v57 offset:20480\n"
    "ds_read_b64_tr_b16 v[42:43], v57 offset:22528\n"
    "ds_read_b128 v[32:35], v54 offset:0\n"
    "s_waitcnt lgkmcnt(7)\n"
    "v_mfma_f32_16x16x32_bf16 v[16:19], v[44:47], v[24:27], 0\n"
    "v_mfma_f32_16x16x32_bf16 v[20:23], v[48:51], v[24:27], 0\n"
    "ds_read_b64_tr_b16 v[44:45], v58 offset:20480\n"
    "ds_read_b64_tr_b16 v[46:47], v58 offset:22528\n"
    "ds_read_b64_tr_b16 v[48:49], v59 offset:20480\n"
    "ds_read_b64_tr_b16 v[50:51], v59 offset:22528\n"
    "s_waitcnt lgkmcnt(9)\n"
    "v_max_f32_dpp v61, v62, v62 row_ror:4 row_mask:0xf bank_mask:0xf\n"
    "s_nop 1\n"
    "v_max_f32_dpp v61, v61, v61 row_ror:8 row_mask:0xf bank_mask:0xf\n"
    "s_waitcnt lgkmcnt(10)\n"
    "s_waitcnt lgkmcnt(5)\n"
    "v_mfma_f32_16x16x32_bf16 v[8:11], v[36:39], v[28:31], v[8:11]\n"
    "v_mfma_f32_16x16x32_bf16 v[12:15], v[40:43], v[28:31], v[12:15]\n"
    "ds_read_b64_tr_b16 v[36:37], v56 offset:24576\n"
    "ds_read_b64_tr_b16 v[38:39], v56 offset:26624\n"
    "ds_read_b64_tr_b16 v[40:41], v57 offset:24576\n"
    "ds_read_b64_tr_b16 v[42:43], v57 offset:26624\n"
    "ds_read_b128 v[24:27], v55 offset:0\n"
    "v_sub_f32 v60, v62, v61\n"
    "v_exp_f32 v60, v60\n"
    "s_waitcnt lgkmcnt(5)\n"
    "v_mfma_f32_16x16x32_bf16 v[16:19], v[44:47], v[28:31], v[16:19]\n"
    "v_mfma_f32_16x16x32_bf16 v[20:23], v[48:51], v[28:31], v[20:23]\n"
    "ds_read_b64_tr_b16 v[44:45], v58 offset:24576\n"
    "ds_read_b64_tr_b16 v[46:47], v58 offset:26624\n"
    "ds_read_b64_tr_b16 v[48:49], v59 offset:24576\n"
    "ds_read_b64_tr_b16 v[50:51], v59 offset:26624\n"
    "v_mov_b32 v62, v61\n"
    "v_mul_f32 v63, v63, v60\n"
    "s_waitcnt lgkmcnt(13)\n"
    "s_waitcnt lgkmcnt(5)\n"
    "v_mfma_f32_16x16x32_bf16 v[8:11], v[36:39], v[32:35], v[8:11]\n"
    "v_mfma_f32_16x16x32_bf16 v[12:15], v[40:43], v[32:35], v[12:15]\n"
    "ds_read_b64_tr_b16 v[36:37], v56 offset:28672\n"
    "ds_read_b64_tr_b16 v[38:39], v56 offset:30720\n"
    "ds_read_b64_tr_b16 v[40:41], v57 offset:28672\n"
    "ds_read_b64_tr_b16 v[42:43], v57 offset:30720\n"
    "v_lshlrev_b32 v61, 8, v0\n"
    "v_add_f32_dpp v63, v63, v63 row_ror:4 row_mask:0xf bank_mask:0xf\n"
    "s_waitcnt lgkmcnt(4)\n"
    "v_mfma_f32_16x16x32_bf16 v[16:19], v[44:47], v[32:35], v[16:19]\n"
    "v_mfma_f32_16x16x32_bf16 v[20:23], v[48:51], v[32:35], v[20:23]\n"
    "ds_read_b64_tr_b16 v[44:45], v58 offset:28672\n"
    "ds_read_b64_tr_b16 v[46:47], v58 offset:30720\n"
    "ds_read_b64_tr_b16 v[48:49], v59 offset:28672\n"
    "ds_read_b64_tr_b16 v[50:51], v59 offset:30720\n"
    "v_min_u32 v62, -2, v62\n"
    "v_add_f32_dpp v63, v63, v63 row_ror:8 row_mask:0xf bank_mask:0xf\n"
    "s_waitcnt lgkmcnt(12)\n"
    "s_waitcnt lgkmcnt(4)\n"
    "v_mfma_f32_16x16x32_bf16 v[8:11], v[36:39], v[24:27], v[8:11]\n"
    "v_mfma_f32_16x16x32_bf16 v[12:15], v[40:43], v[24:27], v[12:15]\n"
    "v_min_u32 v63, -2, v63\n"
    "s_lshl_b32 s54, s45, 2\n"
    "s_add_u32 s54, s54, s44\n"
    "s_lshl_b32 s54, s54, 9\n"
    "s_and_b32 s55, s46, 7\n"
    "s_lshl_b32 s55, s55, 5\n"
    "s_add_u32 s54, s54, s55\n"
    "s_and_b32 s55, s46, 24\n"
    "s_add_u32 s54, s54, s55\n"
    "s_add_u32 s54, s54, s53\n"
    "s_add_u32 s54, s54, 524288\n"
    "s_mov_b64 exec, 3\n"
    "buffer_store_dwordx2 v[62:63], v61, s[4:7], s54 offen sc1\n"
    "s_mov_b64 exec, -1\n"
    "s_waitcnt lgkmcnt(0)\n"
    "v_mfma_f32_16x16x32_bf16 v[16:19], v[44:47], v[24:27], v[16:19]\n"
    "v_mfma_f32_16x16x32_bf16 v[20:23], v[48:51], v[24:27], v[20:23]\n"
    "L_FIN:\n"
    "v_and_b32 v24, 1, v0\n"
    "v_lshlrev_b32 v24, 10, v24\n"
    "v_bfe_u32 v25, v0, 2, 2\n"
    "v_lshl_or_b32 v24, v25, 6, v24\n"
    "v_lshrrev_b32 v25, 4, v0\n"
    "v_lshl_or_b32 v24, v25, 4, v24\n"
    "s_lshl_b32 s54, s45, 5\n"
    "s_add_u32 s54, s54, s46\n"
    "s_lshl_b32 s54, s54, 11\n"
    "s_lshl_b32 s55, s44, 8\n"
    "s_add_u32 s54, s54, s55\n"
    "s_add_u32 s54, s54, s53\n"
    "v_pk_mul_f32 v[8:9], v[8:9], v[60:61] op_sel:[0,0] op_sel_hi:[1,0]\n"
    "v_pk_mul_f32 v[10:11], v[10:11], v[60:61] op_sel:[0,0] op_sel_hi:[1,0]\n"
    "v_pk_mul_f32 v[12:13], v[12:13], v[60:61] op_sel:[0,0] op_sel_hi:[1,0]\n"
    "v_pk_mul_f32 v[14:15], v[14:15], v[60:61] op_sel:[0,0] op_sel_hi:[1,0]\n"
    "v_pk_mul_f32 v[16:17], v[16:17], v[60:61] op_sel:[0,0] op_sel_hi:[1,0]\n"
    "v_pk_mul_f32 v[18:19], v[18:19], v[60:61] op_sel:[0,0] op_sel_hi:[1,0]\n"
    "v_pk_mul_f32 v[20:21], v[20:21], v[60:61] op_sel:[0,0] op_sel_hi:[1,0]\n"
    "v_pk_mul_f32 v[22:23], v[22:23], v[60:61] op_sel:[0,0] op_sel_hi:[1,0]\n"
    "v_add_f32_dpp v8, v8, v8 row_ror:8 row_mask:0xf bank_mask:0x3\n"
    "v_add_f32_dpp v9, v9, v9 row_ror:8 row_mask:0xf bank_mask:0x3\n"
    "v_add_f32_dpp v10, v10, v10 row_ror:8 row_mask:0xf bank_mask:0x3\n"
    "v_add_f32_dpp v11, v11, v11 row_ror:8 row_mask:0xf bank_mask:0x3\n"
    "v_add_f32_dpp v12, v12, v12 row_ror:8 row_mask:0xf bank_mask:0x3\n"
    "v_add_f32_dpp v13, v13, v13 row_ror:8 row_mask:0xf bank_mask:0x3\n"
    "v_add_f32_dpp v14, v14, v14 row_ror:8 row_mask:0xf bank_mask:0x3\n"
    "v_add_f32_dpp v15, v15, v15 row_ror:8 row_mask:0xf bank_mask:0x3\n"
    "v_add_f32_dpp v8, v16, v16 row_ror:8 row_mask:0xf bank_mask:0xc\n"
    "v_add_f32_dpp v9, v17, v17 row_ror:8 row_mask:0xf bank_mask:0xc\n"
    "v_add_f32_dpp v10, v18, v18 row_ror:8 row_mask:0xf bank_mask:0xc\n"
    "v_add_f32_dpp v11, v19, v19 row_ror:8 row_mask:0xf bank_mask:0xc\n"
    "v_add_f32_dpp v12, v20, v20 row_ror:8 row_mask:0xf bank_mask:0xc\n"
    "v_add_f32_dpp v13, v21, v21 row_ror:8 row_mask:0xf bank_mask:0xc\n"
    "v_add_f32_dpp v14, v22, v22 row_ror:8 row_mask:0xf bank_mask:0xc\n"
    "v_add_f32_dpp v15, v23, v23 row_ror:8 row_mask:0xf bank_mask:0xc\n"
    "v_add_f32_dpp v8, v8, v8 row_shl:4 row_mask:0xf bank_mask:0x5\n"
    "v_add_f32_dpp v9, v9, v9 row_shl:4 row_mask:0xf bank_mask:0x5\n"
    "v_add_f32_dpp v10, v10, v10 row_shl:4 row_mask:0xf bank_mask:0x5\n"
    "v_add_f32_dpp v11, v11, v11 row_shl:4 row_mask:0xf bank_mask:0x5\n"
    "v_add_f32_dpp v8, v12, v12 row_shr:4 row_mask:0xf bank_mask:0xa\n"
    "v_add_f32_dpp v9, v13, v13 row_shr:4 row_mask:0xf bank_mask:0xa\n"
    "v_add_f32_dpp v10, v14, v14 row_shr:4 row_mask:0xf bank_mask:0xa\n"
    "v_add_f32_dpp v11, v15, v15 row_shr:4 row_mask:0xf bank_mask:0xa\n"
    "v_add_f32_dpp v8, v8, v8 quad_perm:[2,3,0,1] row_mask:0xf bank_mask:0xf\n"
    "v_add_f32_dpp v9, v9, v9 quad_perm:[2,3,0,1] row_mask:0xf bank_mask:0xf\n"
    "v_add_f32_dpp v10, v10, v10 quad_perm:[2,3,0,1] row_mask:0xf bank_mask:0xf\n"
    "v_add_f32_dpp v11, v11, v11 quad_perm:[2,3,0,1] row_mask:0xf bank_mask:0xf\n"
    "v_min_u32 v8, -2, v8\n"
    "v_min_u32 v9, -2, v9\n"
    "v_min_u32 v10, -2, v10\n"
    "v_min_u32 v11, -2, v11\n"
    "s_mov_b64 exec, s[76:77]\n"
    "buffer_store_dwordx4 v[8:11], v24, s[4:7], s54 offen sc1\n"
    "s_mov_b64 exec, -1\n"
    "s_memrealtime s[64:65]\n"
    "s_bitcmp1_b32 s47, 0\n"
    "s_cselect_b64 s[20:21], s[40:41], s[38:39]\n"
    "s_cselect_b64 s[16:17], s[34:35], s[36:37]\n"
    "s_cselect_b64 s[12:13], s[30:31], s[32:33]\n"
    "s_mov_b32 s55, 135168\n"
    "s_cselect_b32 s69, 65536, s55\n"
    "v_lshrrev_b32 v36, 3, v0\n"
    "v_mul_u32_u24 v36, 3584, v36\n"
    "v_and_b32 v57, 7, v0\n"
    "v_lshl_add_u32 v36, v57, 4, v36\n"
    "v_cmp_eq_u32 vcc, 7, v57\n"
    "v_mov_b32 v57, 1073741824\n"
    "s_nop 0\n"
    "v_cndmask_b32 v36, v36, v57, vcc\n"
    "buffer_load_dwordx4 v[12:15], v36, s[20:23], s71 offen offset:0 nt\n"
    "buffer_load_dwordx4 v[20:23], v36, s[20:23], s71 offen offset:896 nt\n"
    "buffer_load_dwordx4 v[28:31], v36, s[20:23], s71 offen offset:1792 nt\n"
    "buffer_load_dwordx4 v[36:39], v36, s[20:23], s71 offen offset:2688 nt\n"
    "v_lshrrev_b32 v59, 5, v0\n"
    "v_lshlrev_b32 v59, 2, v59\n"
    "v_and_or_b32 v58, v0, 3, v59\n"
    "v_bfe_u32 v59, v0, 2, 3\n"
    "v_cmp_eq_u32 vcc, 7, v59\n"
    "v_mov_b32 v57, 0x3d000000\n"
    "s_nop 0\n"
    "v_cndmask_b32 v1, 0, v57, vcc\n"
    "s_lshl_b32 s54, s45, 2\n"
    "s_add_u32 s54, s54, s44\n"
    "s_lshl_b32 s54, s54, 9\n"
    "s_lshr_b32 s55, s46, 4\n"
    "s_lshl_b32 s55, s55, 8\n"
    "s_add_u32 s54, s54, s55\n"
    "s_add_u32 s54, s54, s53\n"
    "s_add_u32 s54, s54, 524288\n"
    "s_lshl_b32 s55, s45, 16\n"
    "s_lshr_b32 s56, s46, 3\n"
    "s_lshl_b32 s56, s56, 9\n"
    "s_add_u32 s55, s55, s56\n"
    "s_lshl_b32 s56, s44, 7\n"
    "s_add_u32 s55, s55, s56\n"
    "s_add_u32 s55, s55, s53\n"
    "s_add_u32 s56, s55, 16384\n"
    "s_add_u32 s57, s55, 32768\n"
    "s_add_u32 s58, s55, 49152\n"
    "v_lshlrev_b32 v52, 4, v58\n"
    "v_lshl_or_b32 v52, v59, 11, v52\n"
    "v_and_b32 v56, 31, v0\n"
    "v_lshlrev_b32 v56, 3, v56\n"
    "s_cmp_lg_u32 s47, 0\n"
    "s_cbranch_scc1 L_NOB\n"
    "v_lshrrev_b32 v60, 5, v0\n"
    "v_and_b32 v61, 31, v0\n"
    "v_xor_b32 v61, v61, v60\n"
    "v_lshlrev_b32 v61, 4, v61\n"
    "v_lshl_or_b32 v40, v60, 9, v61\n"
    "v_xor_b32 v41, 32, v40\n"
    "v_xor_b32 v42, 64, v40\n"
    "v_xor_b32 v43, 96, v40\n"
    "v_xor_b32 v44, 128, v40\n"
    "v_xor_b32 v45, 160, v40\n"
    "v_xor_b32 v46, 192, v40\n"
    "v_xor_b32 v47, 224, v40\n"
    "v_bfe_u32 v60, v0, 1, 2\n"
    "v_bfe_u32 v61, v0, 4, 2\n"
    "v_xor_b32 v60, v60, v61\n"
    "v_lshlrev_b32 v60, 5, v60\n"
    "v_and_b32 v61, 1, v0\n"
    "v_lshl_or_b32 v60, v61, 4, v60\n"
    "v_lshrrev_b32 v61, 3, v0\n"
    "v_lshl_or_b32 v62, v61, 9, v60\n"
    "s_add_u32 m0, s52, 0\n"
    "s_add_u32 s63, s78, 0\n"
    "buffer_load_dwordx4 v40, s[8:11], s63 offen offset:0 nt lds\n"
    "s_add_u32 m0, s52, 8192\n"
    "s_add_u32 s63, s78, 8192\n"
    "buffer_load_dwordx4 v40, s[8:11], s63 offen offset:0 nt lds\n"
    "s_add_u32 m0, s52, 4096\n"
    "s_add_u32 s63, s78, 4096\n"
    "buffer_load_dwordx4 v44, s[8:11], s63 offen offset:0 nt lds\n"
    "s_add_u32 m0, s52, 12288\n"
    "s_add_u32 s63, s78, 12288\n"
    "buffer_load_dwordx4 v44, s[8:11], s63 offen offset:0 nt lds\n"
    "s_add_u32 m0, s52, 0\n"
    "s_add_u32 s63, s78, 0\n"
    "buffer_load_dwordx4 v41, s[8:11], s63 offen offset:1024 nt lds\n"
    "s_add_u32 m0, s52, 8192\n"
    "s_add_u32 s63, s78, 8192\n"
    "buffer_load_dwordx4 v41, s[8:11], s63 offen offset:1024 nt lds\n"
    "s_add_u32 m0, s52, 4096\n"
    "s_add_u32 s63, s78, 4096\n"
    "buffer_load_dwordx4 v45, s[8:11], s63 offen offset:1024 nt lds\n"
    "s_add_u32 m0, s52, 12288\n"
    "s_add_u32 s63, s78, 12288\n"
    "buffer_load_dwordx4 v45, s[8:11], s63 offen offset:1024 nt lds\n"
    "s_add_u32 m0, s52, 0\n"
    "s_add_u32 s63, s78, 0\n"
    "buffer_load_dwordx4 v42, s[8:11], s63 offen offset:2048 nt lds\n"
    "s_add_u32 m0, s52, 8192\n"
    "s_add_u32 s63, s78, 8192\n"
    "buffer_load_dwordx4 v42, s[8:11], s63 offen offset:2048 nt lds\n"
    "s_add_u32 m0, s52, 4096\n"
    "s_add_u32 s63, s78, 4096\n"
    "buffer_load_dwordx4 v46, s[8:11], s63 offen offset:2048 nt lds\n"
    "s_add_u32 m0, s52, 12288\n"
    "s_add_u32 s63, s78, 12288\n"
    "buffer_load_dwordx4 v46, s[8:11], s63 offen offset:2048 nt lds\n"
    "s_add_u32 m0, s52, 0\n"
    "s_add_u32 s63, s78, 0\n"
    "buffer_load_dwordx4 v43, s[8:11], s63 offen offset:3072 nt lds\n"
    "s_add_u32 m0, s52, 8192\n"
    "s_add_u32 s63, s78, 8192\n"
    "buffer_load_dwordx4 v43, s[8:11], s63 offen offset:3072 nt lds\n"
    "s_add_u32 m0, s52, 4096\n"
    "s_add_u32 s63, s78, 4096\n"
    "buffer_load_dwordx4 v47, s[8:11], s63 offen offset:3072 nt lds\n"
    "s_add_u32 m0, s52, 12288\n"
    "s_add_u32 s63, s78, 12288\n"
    "buffer_load_dwordx4 v47, s[8:11], s63 offen offset:3072 nt lds\n"
    "s_add_u32 m0, s52, 28672\n"
    "s_add_u32 s63, s51, 49152\n"
    "buffer_load_dwordx4 v62, s[12:15], s63 offen offset:0 nt lds\n"
    "s_add_u32 s63, s51, 52224\n"
    "buffer_load_dwordx4 v62, s[12:15], s63 offen offset:1024 nt lds\n"
    "s_add_u32 s63, s51, 55296\n"
    "buffer_load_dwordx4 v62, s[12:15], s63 offen offset:2048 nt lds\n"
    "s_add_u32 s63, s51, 58368\n"
    "buffer_load_dwordx4 v62, s[12:15], s63 offen offset:3072 nt lds\n"
    "s_waitcnt vmcnt(2)\n"
    "L_NOB:\n"
    "s_cmp_lg_u32 s47, 2\n"
    "s_cbranch_scc1 L_NOREST\n"
    "ds_write_b128 v2, a[0:3] offset:16384\n"
    "ds_write_b128 v2, a[4:7] offset:17408\n"
    "ds_write_b128 v2, a[8:11] offset:18432\n"
    "ds_write_b128 v2, a[12:15] offset:19456\n"
    "ds_write_b128 v2, a[16:19] offset:20480\n"
    "ds_write_b128 v2, a[20:23] offset:21504\n"
    "ds_write_b128 v2, a[24:27] offset:22528\n"
    "ds_write_b128 v2, a[28:31] offset:23552\n"
    "ds_write_b128 v2, a[128:131] offset:24576\n"
    "ds_write_b128 v2, a[132:135] offset:25600\n"
    "ds_write_b128 v2, a[136:139] offset:26624\n"
    "ds_write_b128 v2, a[140:143] offset:27648\n"
    "L_NOREST:\n"
    "s_waitcnt lgkmcnt(0)\n"
    "s_add_u32 s67, s64, s80\n"
    "s_sub_u32 s62, s3, s67\n"
    "s_bfe_i32 s62, s62, 1310720\n"
    "s_sub_u32 s62, s62, 1\n"
    "s_cmp_lt_u32 s62, 600\n"
    "s_cselect_b32 s67, s3, s67\n"
    "s_add_u32 s3, s3, s83\n"
    "L_w24:\n"
    "s_memrealtime s[62:63]\n"
    "s_waitcnt lgkmcnt(0)\n"
    "s_sub_u32 s62, s67, s62\n"
    "s_bfe_i32 s62, s62, 1310720\n"
    "s_cmp_gt_i32 s62, 0\n"
    "s_cbranch_scc1 L_w24\n"
    "buffer_load_dwordx2 v[56:57], v56, s[4:7], s54 offen sc1\n"
    "buffer_load_dwordx4 v[40:43], v52, s[4:7], s55 offen sc1\n"
    "buffer_load_dwordx4 v[44:47], v52, s[4:7], s56 offen sc1\n"
    "buffer_load_dwordx4 v[48:51], v52, s[4:7], s57 offen sc1\n"
    "buffer_load_dwordx4 v[52:55], v52, s[4:7], s58 offen sc1\n"
    "s_waitcnt vmcnt(5)\n"
    "v_lshrrev_b32 v9, 5, v0\n"
    "v_lshlrev_b32 v9, 2, v9\n"
    "v_and_or_b32 v8, v0, 3, v9\n"
    "v_bfe_u32 v9, v0, 2, 3\n"
    "v_lshlrev_b32 v9, 2, v9\n"
    "v_lshl_or_b32 v8, v8, 5, v9\n"
    "ds_bpermute_b32 v12, v8, v12\n"
    "ds_bpermute_b32 v13, v8, v13\n"
    "ds_bpermute_b32 v14, v8, v14\n"
    "ds_bpermute_b32 v15, v8, v15\n"
    "ds_bpermute_b32 v20, v8, v20\n"
    "ds_bpermute_b32 v21, v8, v21\n"
    "ds_bpermute_b32 v22, v8, v22\n"
    "ds_bpermute_b32 v23, v8, v23\n"
    "ds_bpermute_b32 v28, v8, v28\n"
    "ds_bpermute_b32 v29, v8, v29\n"
    "ds_bpermute_b32 v30, v8, v30\n"
    "ds_bpermute_b32 v31, v8, v31\n"
    "ds_bpermute_b32 v36, v8, v36\n"
    "ds_bpermute_b32 v37, v8, v37\n"
    "ds_bpermute_b32 v38, v8, v38\n"
    "ds_bpermute_b32 v39, v8, v39\n"
    "s_waitcnt lgkmcnt(0)\n"
    "v_lshlrev_b32 v8, 16, v12\n"
    "v_and_b32 v9, 0xffff0000, v12\n"
    "v_lshlrev_b32 v10, 16, v13\n"
    "v_and_b32 v11, 0xffff0000, v13\n"
    "v_lshlrev_b32 v12, 16, v14\n"
    "v_and_b32 v13, 0xffff0000, v14\n"
    "v_lshlrev_b32 v14, 16, v15\n"
    "v_and_b32 v15, 0xffff0000, v15\n"
    "v_lshlrev_b32 v16, 16, v20\n"
    "v_and_b32 v17, 0xffff0000, v20\n"
    "v_lshlrev_b32 v18, 16, v21\n"
    "v_and_b32 v19, 0xffff0000, v21\n"
    "v_lshlrev_b32 v20, 16, v22\n"
    "v_and_b32 v21, 0xffff0000, v22\n"
    "v_lshlrev_b32 v22, 16, v23\n"
    "v_and_b32 v23, 0xffff0000, v23\n"
    "v_lshlrev_b32 v24, 16, v28\n"
    "v_and_b32 v25, 0xffff0000, v28\n"
    "v_lshlrev_b32 v26, 16, v29\n"
    "v_and_b32 v27, 0xffff0000, v29\n"
    "v_lshlrev_b32 v28, 16, v30\n"
    "v_and_b32 v29, 0xffff0000, v30\n"
    "v_lshlrev_b32 v30, 16, v31\n"
    "v_and_b32 v31, 0xffff0000, v31\n"
    "v_lshlrev_b32 v32, 16, v36\n"
    "v_and_b32 v33, 0xffff0000, v36\n"
    "v_lshlrev_b32 v34, 16, v37\n"
    "v_and_b32 v35, 0xffff0000, v37\n"
    "v_lshlrev_b32 v36, 16, v38\n"
    "v_and_b32 v37, 0xffff0000, v38\n"
    "v_lshlrev_b32 v38, 16, v39\n"
    "v_and_b32 v39, 0xffff0000, v39\n"
    "s_waitcnt vmcnt(4)\n"
    "L_WO_AL:\n"
    "v_max_f32_dpp v59, v56, v56 quad_perm:[1,0,3,2] row_mask:0xf bank_mask:0xf\n"
    "s_nop 1\n"
    "v_max_f32_dpp v59, v59, v59 quad_perm:[2,3,0,1] row_mask:0xf bank_mask:0xf\n"
    "s_nop 1\n"
    "v_max_f32_dpp v59, v59, v59 row_ror:4 row_mask:0xf bank_mask:0xf\n"
    "s_nop 1\n"
    "v_max_f32_dpp v59, v59, v59 row_ror:8 row_mask:0xf bank_mask:0xf\n"
    "v_mov_b32 v60, v59\n"
    "s_nop 1\n"
    "v_permlane16_swap_b32 v59, v60\n"
    "v_max_f32 v59, v59, v60\n"
    "v_sub_f32 v58, v56, v59\n"
    "v_exp_f32 v58, v58\n"
    "s_nop 0\n"
    "v_mul_f32 v59, v58, v57\n"
    "s_nop 1\n"
    "v_add_f32_dpp v59, v59, v59 quad_perm:[1,0,3,2] row_mask:0xf bank_mask:0xf\n"
    "s_nop 1\n"
    "v_add_f32_dpp v59, v59, v59 quad_perm:[2,3,0,1] row_mask:0xf bank_mask:0xf\n"
    "s_nop 1\n"
    "v_add_f32_dpp v59, v59, v59 row_ror:4 row_mask:0xf bank_mask:0xf\n"
    "s_nop 1\n"
    "v_add_f32_dpp v59, v59, v59 row_ror:8 row_mask:0xf bank_mask:0xf\n"
    "v_mov_b32 v60, v59\n"
    "s_nop 1\n"
    "v_permlane16_swap_b32 v59, v60\n"
    "v_add_f32 v59, v59, v60\n"
    "v_rcp_f32 v59, v59\n"
    "s_nop 0\n"
    "v_mul_f32 v58, v58, v59\n"
    "s_waitcnt vmcnt(0)\n"
    "s_nop 0\n"
    "v_mul_f32_dpp v60, v58, v40 quad_perm:[0,0,0,0] row_mask:0xf bank_mask:0xf\n"
    "v_mul_f32_dpp v61, v58, v41 quad_perm:[0,0,0,0] row_mask:0xf bank_mask:0xf\n"
    "v_mul_f32_dpp v62, v58, v42 quad_perm:[0,0,0,0] row_mask:0xf bank_mask:0xf\n"
    "v_mul_f32_dpp v63, v58, v43 quad_perm:[0,0,0,0] row_mask:0xf bank_mask:0xf\n"
    "v_fmac_f32_dpp v60, v58, v44 quad_perm:[1,1,1,1] row_mask:0xf bank_mask:0xf\n"
    "v_fmac_f32_dpp v61, v58, v45 quad_perm:[1,1,1,1] row_mask:0xf bank_mask:0xf\n"
    "v_fmac_f32_dpp v62, v58, v46 quad_perm:[1,1,1,1] row_mask:0xf bank_mask:0xf\n"
    "v_fmac_f32_dpp v63, v58, v47 quad_perm:[1,1,1,1] row_mask:0xf bank_mask:0xf\n"
    "v_fmac_f32_dpp v60, v58, v48 quad_perm:[2,2,2,2] row_mask:0xf bank_mask:0xf\n"
    "v_fmac_f32_dpp v61, v58, v49 quad_perm:[2,2,2,2] row_mask:0xf bank_mask:0xf\n"
    "v_fmac_f32_dpp v62, v58, v50 quad_perm:[2,2,2,2] row_mask:0xf bank_mask:0xf\n"
    "v_fmac_f32_dpp v63, v58, v51 quad_perm:[2,2,2,2] row_mask:0xf bank_mask:0xf\n"
    "v_fmac_f32_dpp v60, v58, v52 quad_perm:[3,3,3,3] row_mask:0xf bank_mask:0xf\n"
    "v_fmac_f32_dpp v61, v58, v53 quad_perm:[3,3,3,3] row_mask:0xf bank_mask:0xf\n"
    "v_fmac_f32_dpp v62, v58, v54 quad_perm:[3,3,3,3] row_mask:0xf bank_mask:0xf\n"
    "v_fmac_f32_dpp v63, v58, v55 quad_perm:[3,3,3,3] row_mask:0xf bank_mask:0xf\n"
    "v_cmp_u_f32 vcc, v60, v61\n"
    "v_cmp_u_f32 s[60:61], v62, v63\n"
    "s_or_b64 vcc, vcc, s[60:61]\n"
    "s_cbranch_scc1 L_WO_CHK\n"
    "L_WO_OK:\n"
    "s_memrealtime s[60:61]\n"
    "v_add_f32_dpp v60, v60, v60 row_ror:4 row_mask:0xf bank_mask:0xf\n"
    "v_add_f32_dpp v61, v61, v61 row_ror:4 row_mask:0xf bank_mask:0xf\n"
    "v_add_f32_dpp v62, v62, v62 row_ror:4 row_mask:0xf bank_mask:0xf\n"
    "v_add_f32_dpp v63, v63, v63 row_ror:4 row_mask:0xf bank_mask:0xf\n"
    "v_add_f32_dpp v60, v60, v60 row_ror:8 row_mask:0xf bank_mask:0xf\n"
    "v_add_f32_dpp v61, v61, v61 row_ror:8 row_mask:0xf bank_mask:0xf\n"
    "v_add_f32_dpp v62, v62, v62 row_ror:8 row_mask:0xf bank_mask:0xf\n"
    "v_add_f32_dpp v63, v63, v63 row_ror:8 row_mask:0xf bank_mask:0xf\n"
    "v_mov_b32 v48, v60\n"
    "v_mov_b32 v49, v61\n"
    "v_mov_b32 v50, v62\n"
    "v_mov_b32 v51, v63\n"
    "v_permlane16_swap_b32 v60, v48\n"
    "v_permlane16_swap_b32 v61, v49\n"
    "v_permlane16_swap_b32 v62, v50\n"
    "v_permlane16_swap_b32 v63, v51\n"
    "v_pk_add_f32 v[60:61], v[60:61], v[48:49]\n"
    "v_pk_add_f32 v[62:63], v[62:63], v[50:51]\n"
    "s_waitcnt lgkmcnt(0)\n"
    "s_and_b32 s60, s60, 0xfffff\n"
    "v_cvt_f32_u32 v58, s60\n"
    "v_mul_f32 v58, v58, v1\n"
    "v_pk_fma_f32 v[40:41], v[8:9], v[60:61], v[58:59] op_sel:[0,0,0] op_sel_hi:[1,0,0]\n"
    "v_pk_fma_f32 v[42:43], v[10:11], v[60:61], v[58:59] op_sel:[0,0,0] op_sel_hi:[1,0,0]\n"
    "v_pk_fma_f32 v[44:45], v[12:13], v[60:61], v[58:59] op_sel:[0,0,0] op_sel_hi:[1,0,0]\n"
    "v_pk_fma_f32 v[46:47], v[14:15], v[60:61], v[58:59] op_sel:[0,0,0] op_sel_hi:[1,0,0]\n"
    "v_pk_fma_f32 v[40:41], v[16:17], v[60:61], v[40:41] op_sel:[0,1,0] op_sel_hi:[1,1,1]\n"
    "v_pk_fma_f32 v[42:43], v[18:19], v[60:61], v[42:43] op_sel:[0,1,0] op_sel_hi:[1,1,1]\n"
    "v_pk_fma_f32 v[44:45], v[20:21], v[60:61], v[44:45] op_sel:[0,1,0] op_sel_hi:[1,1,1]\n"
    "v_pk_fma_f32 v[46:47], v[22:23], v[60:61], v[46:47] op_sel:[0,1,0] op_sel_hi:[1,1,1]\n"
    "v_pk_fma_f32 v[40:41], v[24:25], v[62:63], v[40:41] op_sel:[0,0,0] op_sel_hi:[1,0,1]\n"
    "v_pk_fma_f32 v[42:43], v[26:27], v[62:63], v[42:43] op_sel:[0,0,0] op_sel_hi:[1,0,1]\n"
    "v_pk_fma_f32 v[44:45], v[28:29], v[62:63], v[44:45] op_sel:[0,0,0] op_sel_hi:[1,0,1]\n"
    "v_pk_fma_f32 v[46:47], v[30:31], v[62:63], v[46:47] op_sel:[0,0,0] op_sel_hi:[1,0,1]\n"
    "v_pk_fma_f32 v[40:41], v[32:33], v[62:63], v[40:41] op_sel:[0,1,0] op_sel_hi:[1,1,1]\n"
    "v_pk_fma_f32 v[42:43], v[34:35], v[62:63], v[42:43] op_sel:[0,1,0] op_sel_hi:[1,1,1]\n"
    "v_pk_fma_f32 v[44:45], v[36:37], v[62:63], v[44:45] op_sel:[0,1,0] op_sel_hi:[1,1,1]\n"
    "v_pk_fma_f32 v[46:47], v[38:39], v[62:63], v[46:47] op_sel:[0,1,0] op_sel_hi:[1,1,1]\n"
    "v_bfe_u32 v56, v0, 2, 3\n"
    "v_lshlrev_b32 v56, 5, v56\n"
    "v_lshrrev_b32 v57, 5, v0\n"
    "v_lshl_or_b32 v56, v57, 4, v56\n"
    "s_lshl_b32 s60, s44, 8\n"
    "v_add_u32 v56, s60, v56\n"
    "v_permlane32_swap_b32 v40, v44\n"
    "v_permlane32_swap_b32 v41, v45\n"
    "v_permlane32_swap_b32 v42, v46\n"
    "v_permlane32_swap_b32 v43, v47\n"
    "v_pk_add_f32 v[40:41], v[40:41], v[44:45]\n"
    "v_pk_add_f32 v[42:43], v[42:43], v[46:47]\n"
    "s_nop 0\n"
    "v_add_f32_dpp v40, v40, v40 quad_perm:[1,0,3,2] row_mask:0xf bank_mask:0xf\n"
    "v_add_f32_dpp v41, v41, v41 quad_perm:[1,0,3,2] row_mask:0xf bank_mask:0xf\n"
    "v_add_f32_dpp v42, v42, v42 quad_perm:[1,0,3,2] row_mask:0xf bank_mask:0xf\n"
    "v_add_f32_dpp v43, v43, v43 quad_perm:[1,0,3,2] row_mask:0xf bank_mask:0xf\n"
    "v_add_f32_dpp v40, v40, v40 quad_perm:[2,3,0,1] row_mask:0xf bank_mask:0xf\n"
    "v_add_f32_dpp v41, v41, v41 quad_perm:[2,3,0,1] row_mask:0xf bank_mask:0xf\n"
    "v_add_f32_dpp v42, v42, v42 quad_perm:[2,3,0,1] row_mask:0xf bank_mask:0xf\n"
    "v_add_f32_dpp v43, v43, v43 quad_perm:[2,3,0,1] row_mask:0xf bank_mask:0xf\n"
    "ds_write_b128 v56, v[40:43] offset:3456\n"
    "v_lshrrev_b32 v59, 5, v0\n"
    "v_lshlrev_b32 v59, 2, v59\n"
    "v_and_or_b32 v58, v0, 3, v59\n"
    "v_bfe_u32 v59, v0, 2, 3\n"
    "v_lshrrev_b32 v36, 3, v0\n"
    "v_lshlrev_b32 v36, 15, v36\n"
    "v_and_b32 v57, 7, v0\n"
    "v_lshl_or_b32 v36, v57, 4, v36\n"
    "v_cmp_lt_u32 vcc, 55, v0\n"
    "v_mov_b32 v57, 1073741824\n"
    "s_nop 0\n"
    "v_cndmask_b32 v36, v36, v57, vcc\n"
    "v_lshlrev_b32 v52, 4, v58\n"
    "v_lshl_or_b32 v52, v59, 9, v52\n"
    "s_waitcnt lgkmcnt(0)\n"
    "s_barrier\n"
    "s_cmp_lg_u32 s44, 0\n"
    "s_cbranch_scc1 L_WO_PUBD\n"
    "v_lshlrev_b32 v56, 4, v0\n"
    "ds_read_b128 v[8:11], v56 offset:3456\n"
    "ds_read_b128 v[12:15], v56 offset:3712\n"
    "ds_read_b128 v[16:19], v56 offset:3968\n"
    "ds_read_b128 v[20:23], v56 offset:4224\n"
    "s_bfe_u32 s54, s46, 131073\n"
    "s_lshl_b32 s54, s54, 5\n"
    "s_lshl_b32 s55, s45, 2\n"
    "s_add_u32 s54, s54, s55\n"
    "s_lshr_b32 s55, s46, 3\n"
    "s_add_u32 s54, s54, s55\n"
    "s_lshl_b32 s54, s54, 9\n"
    "s_and_b32 s55, s46, 1\n"
    "s_lshl_b32 s55, s55, 8\n"
    "s_add_u32 s54, s54, s55\n"
    "s_add_u32 s54, s54, s53\n"
    "s_add_u32 s54, s54, 540672\n"
    "s_waitcnt lgkmcnt(0)\n"
    "v_pk_add_f32 v[8:9], v[8:9], v[12:13]\n"
    "v_pk_add_f32 v[10:11], v[10:11], v[14:15]\n"
    "v_pk_add_f32 v[16:17], v[16:17], v[20:21]\n"
    "v_pk_add_f32 v[18:19], v[18:19], v[22:23]\n"
    "v_pk_add_f32 v[8:9], v[8:9], v[16:17]\n"
    "v_pk_add_f32 v[10:11], v[10:11], v[18:19]\n"
    "v_min_u32 v8, -2, v8\n"
    "v_min_u32 v9, -2, v9\n"
    "v_min_u32 v10, -2, v10\n"
    "v_min_u32 v11, -2, v11\n"
    "s_bfm_b64 exec, 15, 0\n"
    "buffer_store_dwordx4 v[8:11], v56, s[4:7], s54 offen sc1\n"
    "s_mov_b64 exec, -1\n"
    "L_WO_PUBD:\n"
    "s_memrealtime s[64:65]\n"
    "s_cmp_lg_u32 s47, 3\n"
    "s_cbranch_scc1 L_WQ\n"
    "s_and_b32 s54, s2, 63\n"
    "s_cmp_lg_u32 s54, 0\n"
    "s_cbranch_scc1 L_END\n"
    "L_WQ:\n"
    "s_add_u32 s63, s72, 0\n"
    "buffer_load_dwordx4 v[12:15], v36, s[16:19], s63 offen nt\n"
    "s_add_u32 s63, s72, 8192\n"
    "buffer_load_dwordx4 v[20:23], v36, s[16:19], s63 offen nt\n"
    "s_add_u32 s63, s72, 16384\n"
    "buffer_load_dwordx4 v[28:31], v36, s[16:19], s63 offen nt\n"
    "s_add_u32 s63, s72, 24576\n"
    "buffer_load_dwordx4 v[36:39], v36, s[16:19], s63 offen nt\n"
    "v_lshlrev_b32 v62, 3, v0\n"
    "s_lshl_b32 s54, s45, 13\n"
    "s_lshl_b32 s55, s44, 9\n"
    "s_add_u32 s54, s54, s55\n"
    "s_add_u32 s54, s54, s69\n"
    "s_add_u32 s55, s54, 4096\n"
    "buffer_load_dwordx2 v[56:57], v62, s[4:7], s54 offen offset:0 sc1\n"
    "buffer_load_dwordx2 v[58:59], v62, s[4:7], s54 offen offset:2048 sc1\n"
    "buffer_load_dwordx2 v[60:61], v62, s[4:7], s55 offen offset:0 sc1\n"
    "buffer_load_dwordx2 v[62:63], v62, s[4:7], s55 offen offset:2048 sc1\n"
    "s_lshr_b32 s55, s46, 3\n"
    "s_lshl_b32 s55, s55, 14\n"
    "s_lshr_b32 s56, s44, 1\n"
    "s_lshl_b32 s56, s56, 8\n"
    "s_add_u32 s55, s55, s56\n"
    "s_and_b32 s56, s44, 1\n"
    "s_mul_i32 s56, s56, 112\n"
    "s_add_u32 s55, s55, s56\n"
    "s_add_u32 s55, s55, s53\n"
    "s_add_u32 s55, s55, 540672\n"
    "s_add_u32 s56, s55, 4096\n"
    "s_add_u32 s57, s55, 8192\n"
    "s_add_u32 s58, s55, 12288\n"
    "s_add_u32 s68, s53, 606208\n"
    "s_waitcnt lgkmcnt(0)\n"
    "s_add_u32 s67, s64, s81\n"
    "s_sub_u32 s62, s3, s67\n"
    "s_bfe_i32 s62, s62, 1310720\n"
    "s_sub_u32 s62, s62, 1\n"
    "s_cmp_lt_u32 s62, 600\n"
    "s_cselect_b32 s67, s3, s67\n"
    "s_add_u32 s3, s3, s84\n"
    "L_w25:\n"
    "s_memrealtime s[62:63]\n"
    "s_waitcnt lgkmcnt(0)\n"
    "s_sub_u32 s62, s67, s62\n"
    "s_bfe_i32 s62, s62, 1310720\n"
    "s_cmp_gt_i32 s62, 0\n"
    "s_cbranch_scc1 L_w25\n"
    "buffer_load_dwordx4 v[40:43], v52, s[4:7], s55 offen sc1\n"
    "buffer_load_dwordx4 v[44:47], v52, s[4:7], s56 offen sc1\n"
    "buffer_load_dwordx4 v[48:51], v52, s[4:7], s57 offen sc1\n"
    "buffer_load_dwordx4 v[52:55], v52, s[4:7], s58 offen sc1\n"
    "s_waitcnt vmcnt(4)\n"
    "v_lshrrev_b32 v9, 5, v0\n"
    "v_lshlrev_b32 v9, 2, v9\n"
    "v_and_or_b32 v8, v0, 3, v9\n"
    "v_bfe_u32 v9, v0, 2, 3\n"
    "v_lshlrev_b32 v9, 2, v9\n"
    "v_lshl_or_b32 v8, v8, 5, v9\n"
    "ds_bpermute_b32 v12, v8, v12\n"
    "ds_bpermute_b32 v13, v8, v13\n"
    "ds_bpermute_b32 v14, v8, v14\n"
    "ds_bpermute_b32 v15, v8, v15\n"
    "ds_bpermute_b32 v20, v8, v20\n"
    "ds_bpermute_b32 v21, v8, v21\n"
    "ds_bpermute_b32 v22, v8, v22\n"
    "ds_bpermute_b32 v23, v8, v23\n"
    "ds_bpermute_b32 v28, v8, v28\n"
    "ds_bpermute_b32 v29, v8, v29\n"
    "ds_bpermute_b32 v30, v8, v30\n"
    "ds_bpermute_b32 v31, v8, v31\n"
    "ds_bpermute_b32 v36, v8, v36\n"
    "ds_bpermute_b32 v37, v8, v37\n"
    "ds_bpermute_b32 v38, v8, v38\n"
    "ds_bpermute_b32 v39, v8, v39\n"
    "s_waitcnt lgkmcnt(0)\n"
    "v_lshlrev_b32 v8, 16, v12\n"
    "v_and_b32 v9, 0xffff0000, v12\n"
    "v_lshlrev_b32 v10, 16, v13\n"
    "v_and_b32 v11, 0xffff0000, v13\n"
    "v_lshlrev_b32 v12, 16, v14\n"
    "v_and_b32 v13, 0xffff0000, v14\n"
    "v_lshlrev_b32 v14, 16, v15\n"
    "v_and_b32 v15, 0xffff0000, v15\n"
    "v_lshlrev_b32 v16, 16, v20\n"
    "v_and_b32 v17, 0xffff0000, v20\n"
    "v_lshlrev_b32 v18, 16, v21\n"
    "v_and_b32 v19, 0xffff0000, v21\n"
    "v_lshlrev_b32 v20, 16, v22\n"
    "v_and_b32 v21, 0xffff0000, v22\n"
    "v_lshlrev_b32 v22, 16, v23\n"
    "v_and_b32 v23, 0xffff0000, v23\n"
    "v_lshlrev_b32 v24, 16, v28\n"
    "v_and_b32 v25, 0xffff0000, v28\n"
    "v_lshlrev_b32 v26, 16, v29\n"
    "v_and_b32 v27, 0xffff0000, v29\n"
    "v_lshlrev_b32 v28, 16, v30\n"
    "v_and_b32 v29, 0xffff0000, v30\n"
    "v_lshlrev_b32 v30, 16, v31\n"
    "v_and_b32 v31, 0xffff0000, v31\n"
    "v_lshlrev_b32 v32, 16, v36\n"
    "v_and_b32 v33, 0xffff0000, v36\n"
    "v_lshlrev_b32 v34, 16, v37\n"
    "v_and_b32 v35, 0xffff0000, v37\n"
    "v_lshlrev_b32 v36, 16, v38\n"
    "v_and_b32 v37, 0xffff0000, v38\n"
    "v_lshlrev_b32 v38, 16, v39\n"
    "v_and_b32 v39, 0xffff0000, v39\n"
    "v_pk_add_f32 v[56:57], v[56:57], v[58:59]\n"
    "v_pk_add_f32 v[60:61], v[60:61], v[62:63]\n"
    "v_pk_add_f32 v[2:3], v[56:57], v[60:61]\n"
    "s_waitcnt vmcnt(0)\n"
    "L_WQ_GOT:\n"
    "v_pk_add_f32 v[56:57], v[40:41], v[44:45]\n"
    "v_pk_add_f32 v[58:59], v[42:43], v[46:47]\n"
    "v_pk_add_f32 v[60:61], v[48:49], v[52:53]\n"
    "v_pk_add_f32 v[62:63], v[50:51], v[54:55]\n"
    "v_pk_add_f32 v[56:57], v[56:57], v[60:61]\n"
    "v_pk_add_f32 v[58:59], v[58:59], v[62:63]\n"
    "v_max3_f32 v1, v40, v44, v48\n"
    "v_cmp_u_f32 vcc, v56, v57\n"
    "v_cmp_u_f32 s[60:61], v58, v59\n"
    "s_or_b64 vcc, vcc, s[60:61]\n"
    "v_max_f32 v1, v1, v52\n"
    "s_cbranch_scc1 L_WQ_CHK\n"
    "L_WQ_OK:\n"
    "v_add_f32_dpp v56, v56, v56 row_ror:4 row_mask:0xf bank_mask:0xf\n"
    "v_add_f32_dpp v57, v57, v57 row_ror:4 row_mask:0xf bank_mask:0xf\n"
    "v_add_f32_dpp v58, v58, v58 row_ror:4 row_mask:0xf bank_mask:0xf\n"
    "v_add_f32_dpp v59, v59, v59 row_ror:4 row_mask:0xf bank_mask:0xf\n"
    "v_add_f32_dpp v56, v56, v56 row_ror:8 row_mask:0xf bank_mask:0xf\n"
    "v_add_f32_dpp v57, v57, v57 row_ror:8 row_mask:0xf bank_mask:0xf\n"
    "v_add_f32_dpp v58, v58, v58 row_ror:8 row_mask:0xf bank_mask:0xf\n"
    "v_add_f32_dpp v59, v59, v59 row_ror:8 row_mask:0xf bank_mask:0xf\n"
    "v_mov_b32 v60, v56\n"
    "v_mov_b32 v61, v57\n"
    "v_mov_b32 v62, v58\n"
    "v_mov_b32 v63, v59\n"
    "v_permlane16_swap_b32 v56, v60\n"
    "v_permlane16_swap_b32 v57, v61\n"
    "v_permlane16_swap_b32 v58, v62\n"
    "v_permlane16_swap_b32 v59, v63\n"
    "v_pk_add_f32 v[56:57], v[56:57], v[60:61]\n"
    "v_pk_add_f32 v[58:59], v[58:59], v[62:63]\n"
    "s_mov_b32 exec_hi, 0x77777777\n"
    "v_pk_add_f32 v[4:5], v[4:5], v[56:57]\n"
    "v_pk_add_f32 v[6:7], v[6:7], v[58:59]\n"
    "s_mov_b32 exec_hi, -1\n"
    "s_cmp_eq_u32 s47, 3\n"
    "s_cbranch_scc1 L_OUT\n"
    "v_mov_b32 v60, v1\n"
    "L_WQ_GEMV:\n"
    "v_pk_mul_f32 v[40:41], v[8:9], v[4:5] op_sel:[0,0] op_sel_hi:[1,0]\n"
    "v_pk_mul_f32 v[42:43], v[10:11], v[4:5] op_sel:[0,0] op_sel_hi:[1,0]\n"
    "v_pk_mul_f32 v[44:45], v[12:13], v[4:5] op_sel:[0,0] op_sel_hi:[1,0]\n"
    "v_pk_mul_f32 v[46:47], v[14:15], v[4:5] op_sel:[0,0] op_sel_hi:[1,0]\n"
    "v_pk_fma_f32 v[40:41], v[16:17], v[4:5], v[40:41] op_sel:[0,1,0] op_sel_hi:[1,1,1]\n"
    "v_pk_fma_f32 v[42:43], v[18:19], v[4:5], v[42:43] op_sel:[0,1,0] op_sel_hi:[1,1,1]\n"
    "v_pk_fma_f32 v[44:45], v[20:21], v[4:5], v[44:45] op_sel:[0,1,0] op_sel_hi:[1,1,1]\n"
    "v_pk_fma_f32 v[46:47], v[22:23], v[4:5], v[46:47] op_sel:[0,1,0] op_sel_hi:[1,1,1]\n"
    "v_pk_fma_f32 v[40:41], v[24:25], v[6:7], v[40:41] op_sel:[0,0,0] op_sel_hi:[1,0,1]\n"
    "v_pk_fma_f32 v[42:43], v[26:27], v[6:7], v[42:43] op_sel:[0,0,0] op_sel_hi:[1,0,1]\n"
    "v_pk_fma_f32 v[44:45], v[28:29], v[6:7], v[44:45] op_sel:[0,0,0] op_sel_hi:[1,0,1]\n"
    "v_pk_fma_f32 v[46:47], v[30:31], v[6:7], v[46:47] op_sel:[0,0,0] op_sel_hi:[1,0,1]\n"
    "v_pk_fma_f32 v[40:41], v[32:33], v[6:7], v[40:41] op_sel:[0,1,0] op_sel_hi:[1,1,1]\n"
    "v_pk_fma_f32 v[42:43], v[34:35], v[6:7], v[42:43] op_sel:[0,1,0] op_sel_hi:[1,1,1]\n"
    "v_pk_fma_f32 v[44:45], v[36:37], v[6:7], v[44:45] op_sel:[0,1,0] op_sel_hi:[1,1,1]\n"
    "v_pk_fma_f32 v[46:47], v[38:39], v[6:7], v[46:47] op_sel:[0,1,0] op_sel_hi:[1,1,1]\n"
    "v_pk_add_f32 v[56:57], v[4:5], v[6:7]\n"
    "v_pk_mul_f32 v[58:59], v[4:5], v[4:5]\n"
    "v_pk_fma_f32 v[58:59], v[6:7], v[6:7], v[58:59]\n"
    "v_add_f32 v56, v56, v57\n"
    "v_add_f32 v57, v58, v59\n"
    "v_bfe_u32 v58, v0, 2, 3\n"
    "v_lshlrev_b32 v58, 5, v58\n"
    "v_lshrrev_b32 v59, 5, v0\n"
    "v_lshl_or_b32 v58, v59, 4, v58\n"
    "s_mul_i32 s60, s44, 288\n"
    "v_add_u32 v58, s60, v58\n"
    "v_lshlrev_b32 v59, 2, v59\n"
    "v_add_u32 v59, s60, v59\n"
    "v_permlane32_swap_b32 v40, v44\n"
    "v_permlane32_swap_b32 v41, v45\n"
    "v_permlane32_swap_b32 v42, v46\n"
    "v_permlane32_swap_b32 v43, v47\n"
    "v_permlane32_swap_b32 v56, v57\n"
    "v_pk_add_f32 v[40:41], v[40:41], v[44:45]\n"
    "v_pk_add_f32 v[42:43], v[42:43], v[46:47]\n"
    "v_add_f32 v56, v56, v57\n"
    "v_add_f32_dpp v40, v40, v40 quad_perm:[1,0,3,2] row_mask:0xf bank_mask:0xf\n"
    "v_add_f32_dpp v41, v41, v41 quad_perm:[1,0,3,2] row_mask:0xf bank_mask:0xf\n"
    "v_add_f32_dpp v42, v42, v42 quad_perm:[1,0,3,2] row_mask:0xf bank_mask:0xf\n"
    "v_add_f32_dpp v43, v43, v43 quad_perm:[1,0,3,2] row_mask:0xf bank_mask:0xf\n"
    "v_add_f32_dpp v56, v56, v56 quad_perm:[1,0,3,2] row_mask:0xf bank_mask:0xf\n"
    "v_add_f32_dpp v40, v40, v40 quad_perm:[2,3,0,1] row_mask:0xf bank_mask:0xf\n"
    "v_add_f32_dpp v41, v41, v41 quad_perm:[2,3,0,1] row_mask:0xf bank_mask:0xf\n"
    "v_add_f32_dpp v42, v42, v42 quad_perm:[2,3,0,1] row_mask:0xf bank_mask:0xf\n"
    "v_add_f32_dpp v43, v43, v43 quad_perm:[2,3,0,1] row_mask:0xf bank_mask:0xf\n"
    "v_add_f32_dpp v56, v56, v56 quad_perm:[2,3,0,1] row_mask:0xf bank_mask:0xf\n"
    "ds_write_b128 v58, v[40:43] offset:4480\n"
    "ds_write_b32 v59, v56 offset:4736\n"
    "v_max_f32_dpp v60, v60, v60 row_ror:4 row_mask:0xf bank_mask:0xf\n"
    "s_lshr_b32 s61, s44, 1\n"
    "s_lshl_b32 s61, s61, 2\n"
    "s_bitcmp1_b32 s44, 0\n"
    "s_cselect_b32 s61, s61, 8\n"
    "v_max_f32_dpp v60, v60, v60 row_ror:8 row_mask:0xf bank_mask:0xf\n"
    "s_add_u32 s61, s61, s60\n"
    "v_mov_b32 v62, s61\n"
    "v_mov_b32 v61, v60\n"
    "s_nop 1\n"
    "v_permlane16_swap_b32 v60, v61\n"
    "v_max_f32 v60, v60, v61\n"
    "s_bfm_b64 exec, 1, 63\n"
    "ds_write_b32 v62, v60 offset:4744\n"
    "s_mov_b64 exec, -1\n"
    "s_waitcnt lgkmcnt(0)\n"
    "s_barrier\n"
    "s_cmp_lg_u32 s44, 0\n"
    "s_cbranch_scc1 L_WQ_PUBD\n"
    "v_lshlrev_b32 v56, 4, v0\n"
    "ds_read_b128 v[40:43], v56 offset:4480\n"
    "ds_read_b128 v[44:47], v56 offset:4768\n"
    "ds_read_b128 v[48:51], v56 offset:5056\n"
    "ds_read_b128 v[52:55], v56 offset:5344\n"
    "s_lshl_b32 s54, s45, 2\n"
    "s_lshr_b32 s56, s46, 3\n"
    "s_add_u32 s54, s54, s56\n"
    "s_lshl_b32 s54, s54, 11\n"
    "s_and_b32 s57, s46, 7\n"
    "s_lshl_b32 s55, s57, 8\n"
    "s_add_u32 s54, s54, s55\n"
    "s_lshl_b32 s55, s45, 3\n"
    "s_add_u32 s55, s55, s57\n"
    "s_lshl_b32 s55, s55, 6\n"
    "s_lshl_b32 s56, s56, 4\n"
    "s_add_u32 s55, s55, s56\n"
    "s_add_u32 s55, s55, 65536\n"
    "s_sub_u32 s55, s55, s54\n"
    "s_add_u32 s54, s54, s68\n"
    "v_mov_b32 v57, v56\n"
    "v_writelane_b32 v57, s55, 16\n"
    "s_waitcnt lgkmcnt(0)\n"
    "v_pk_add_f32 v[40:41], v[40:41], v[44:45]\n"
    "v_pk_add_f32 v[42:43], v[42:43], v[46:47]\n"
    "v_pk_add_f32 v[48:49], v[48:49], v[52:53]\n"
    "v_pk_add_f32 v[50:51], v[50:51], v[54:55]\n"
    "v_pk_add_f32 v[40:41], v[40:41], v[48:49]\n"
    "v_pk_add_f32 v[42:43], v[42:43], v[50:51]\n"
    "v_min_u32 v40, -2, v40\n"
    "v_min_u32 v41, -2, v41\n"
    "v_min_u32 v42, -2, v42\n"
    "v_min_u32 v43, -2, v43\n"
    "s_bfm_b64 exec, 17, 0\n"
    "buffer_store_dwordx4 v[40:43], v57, s[4:7], s54 offen sc1\n"
    "s_mov_b64 exec, -1\n"
    "L_WQ_PUBD:\n"
    "s_cmp_lg_u32 s48, 0\n"
    "s_cbranch_scc1 L_DISP\n"
    "s_mov_b32 s70, s68\n"
    "s_add_u32 s47, s47, 1\n"
    "s_add_u32 s53, s53, 675840\n"
    "s_branch L_Q\n"
    "L_DISP:\n"
    "s_cmp_eq_u32 s48, 1\n"
    "s_cbranch_scc1 L_RET1\n"
    "s_cmp_eq_u32 s48, 2\n"
    "s_cbranch_scc1 L_RET2\n"
    "s_branch L_RET3\n"
    "L_Q_CHK:\n"
    "s_lshl_b32 s54, s45, 13\n"
    "s_lshl_b32 s55, s44, 9\n"
    "s_add_u32 s54, s54, s55\n"
    "s_add_u32 s54, s54, s70\n"
    "s_add_u32 s55, s54, 4096\n"
    "s_lshl_b32 s56, s45, 3\n"
    "s_and_b32 s63, s46, 7\n"
    "s_add_u32 s56, s56, s63\n"
    "s_lshl_b32 s56, s56, 6\n"
    "s_add_u32 s56, s56, s70\n"
    "s_add_u32 s56, s56, 65536\n"
    "s_mov_b32 s59, 0\n"
    "L_Q_CHK_L:\n"
    "v_max_u32 v27, v28, v29\n"
    "v_cmp_gt_u32 vcc, 4, v0\n"
    "s_nop 1\n"
    "v_cndmask_b32 v27, 0, v27, vcc\n"
    "v_max_u32 v26, v27, v8\n"
    "v_max3_u32 v26, v26, v9, v10\n"
    "v_max3_u32 v26, v26, v11, v12\n"
    "v_max3_u32 v26, v26, v13, v14\n"
    "v_max_u32 v26, v26, v15\n"
    "v_cmp_eq_u32 vcc, -1, v26\n"
    "s_cbranch_vccz L_Q_CHK_X\n"
    "s_sub_u32 s73, s73, 1\n"
    "s_cbranch_scc1 L_Q_CHK_T\n"
    "s_mov_b64 exec, vcc\n"
    "buffer_load_dwordx2 v[8:9], v18, s[4:7], s54 offen offset:0 sc1\n"
    "buffer_load_dwordx2 v[10:11], v18, s[4:7], s54 offen offset:2048 sc1\n"
    "buffer_load_dwordx2 v[12:13], v18, s[4:7], s55 offen offset:0 sc1\n"
    "buffer_load_dwordx2 v[14:15], v18, s[4:7], s55 offen offset:2048 sc1\n"
    "s_and_b64 exec, exec, 15\n"
    "s_cbranch_execz L_Q_CHK_NS\n"
    "buffer_load_dwordx4 v[28:31], v17, s[4:7], s56 offen sc1\n"
    "L_Q_CHK_NS:\n"
    "s_mov_b64 exec, -1\n"
    "s_mov_b32 s59, 1\n"
    "s_waitcnt vmcnt(0)\n"
    "s_branch L_Q_CHK_L\n"
    "L_Q_CHK_X:\n"
    "s_cmp_eq_u32 s59, 0\n"
    "s_cbranch_scc1 L_Q_OK\n"
    "s_branch L_Q_GOT\n"
    "L_Q_CHK_T:\n"
    "s_mov_b32 s73, 0\n"
    "s_branch L_Q_OK\n"
    "L_WO_CHK:\n"
    "v_lshrrev_b32 v59, 5, v0\n"
    "v_lshlrev_b32 v59, 2, v59\n"
    "v_and_or_b32 v58, v0, 3, v59\n"
    "v_bfe_u32 v59, v0, 2, 3\n"
    "v_lshlrev_b32 v2, 4, v58\n"
    "v_lshl_or_b32 v2, v59, 11, v2\n"
    "v_and_b32 v3, 31, v0\n"
    "v_lshlrev_b32 v3, 3, v3\n"
    "s_mov_b32 s59, 0\n"
    "L_WO_CHK_L:\n"
    "v_max_u32 v59, v56, v57\n"
    "v_max3_u32 v59, v59, v40, v41\n"
    "v_max3_u32 v59, v59, v42, v43\n"
    "v_max3_u32 v59, v59, v44, v45\n"
    "v_max3_u32 v59, v59, v46, v47\n"
    "v_max3_u32 v59, v59, v48, v49\n"
    "v_max3_u32 v59, v59, v50, v51\n"
    "v_max3_u32 v59, v59, v52, v53\n"
    "v_max3_u32 v59, v59, v54, v55\n"
    "v_cmp_eq_u32 vcc, -1, v59\n"
    "s_cbranch_vccz L_WO_CHK_X\n"
    "s_sub_u32 s73, s73, 1\n"
    "s_cbranch_scc1 L_WO_CHK_T\n"
    "s_mov_b64 exec, vcc\n"
    "buffer_load_dwordx2 v[56:57], v3, s[4:7], s54 offen sc1\n"
    "buffer_load_dwordx4 v[40:43], v2, s[4:7], s55 offen sc1\n"
    "buffer_load_dwordx4 v[44:47], v2, s[4:7], s56 offen sc1\n"
    "buffer_load_dwordx4 v[48:51], v2, s[4:7], s57 offen sc1\n"
    "buffer_load_dwordx4 v[52:55], v2, s[4:7], s58 offen sc1\n"
    "s_mov_b64 exec, -1\n"
    "s_mov_b32 s59, 1\n"
    "s_waitcnt vmcnt(0)\n"
    "s_branch L_WO_CHK_L\n"
    "L_WO_CHK_X:\n"
    "s_cmp_eq_u32 s59, 0\n"
    "s_cbranch_scc1 L_WO_OK\n"
    "s_branch L_WO_AL\n"
    "L_WO_CHK_T:\n"
    "s_mov_b32 s73, 0\n"
    "s_branch L_WO_OK\n"
    "L_WQ_CHK:\n"
    "v_lshrrev_b32 v63, 5, v0\n"
    "v_lshlrev_b32 v63, 2, v63\n"
    "v_and_or_b32 v62, v0, 3, v63\n"
    "v_bfe_u32 v63, v0, 2, 3\n"
    "v_lshlrev_b32 v61, 4, v62\n"
    "v_lshl_or_b32 v61, v63, 9, v61\n"
    "s_mov_b32 s59, 0\n"
    "L_WQ_CHK_L:\n"
    "v_max_u32 v60, v40, v41\n"
    "v_max3_u32 v60, v60, v42, v43\n"
    "v_max3_u32 v60, v60, v44, v45\n"
    "v_max3_u32 v60, v60, v46, v47\n"
    "v_max3_u32 v60, v60, v48, v49\n"
    "v_max3_u32 v60, v60, v50, v51\n"
    "v_max3_u32 v60, v60, v52, v53\n"
    "v_max3_u32 v60, v60, v54, v55\n"
    "v_cmp_eq_u32 vcc, -1, v60\n"
    "s_cbranch_vccz L_WQ_CHK_X\n"
    "s_sub_u32 s73, s73, 1\n"
    "s_cbranch_scc1 L_WQ_CHK_T\n"
    "s_mov_b64 exec, vcc\n"
    "buffer_load_dwordx4 v[40:43], v61, s[4:7], s55 offen sc1\n"
    "buffer_load_dwordx4 v[44:47], v61, s[4:7], s56 offen sc1\n"
    "buffer_load_dwordx4 v[48:51], v61, s[4:7], s57 offen sc1\n"
    "buffer_load_dwordx4 v[52:55], v61, s[4:7], s58 offen sc1\n"
    "s_mov_b64 exec, -1\n"
    "s_mov_b32 s59, 1\n"
    "s_waitcnt vmcnt(0)\n"
    "s_branch L_WQ_CHK_L\n"
    "L_WQ_CHK_X:\n"
    "s_cmp_eq_u32 s59, 0\n"
    "s_cbranch_scc1 L_WQ_OK\n"
    "s_branch L_WQ_GOT\n"
    "L_WQ_CHK_T:\n"
    "s_mov_b32 s73, 0\n"
    "s_branch L_WQ_OK\n"
    "L_OUT:\n"
    "v_cvt_pk_bf16_f32 v8, v4, v5\n"
    "v_cvt_pk_bf16_f32 v9, v6, v7\n"
    "v_lshrrev_b32 v11, 5, v0\n"
    "v_lshlrev_b32 v11, 2, v11\n"
    "v_and_or_b32 v10, v0, 3, v11\n"
    "v_lshlrev_b32 v10, 3, v10\n"
    "s_mov_b64 s[24:25], s[42:43]\n"
    "s_lshr_b32 s54, s46, 3\n"
    "s_mul_i32 s54, s54, 224\n"
    "s_mul_i32 s55, s44, 56\n"
    "s_add_u32 s54, s54, s55\n"
    "s_mov_b32 exec_lo, 15\n"
    "s_mov_b32 exec_hi, 7\n"
    "buffer_store_dwordx2 v[8:9], v10, s[24:27], s54 offen\n"
    "s_mov_b64 exec, -1\n"
    "L_END:\n"
    "s_waitcnt vmcnt(0) lgkmcnt(0)\n"
    "L_CODE1:\n"
    : "+{v0}"(tid)
    : "{s[0:1]}"(__builtin_amdgcn_kernarg_segment_ptr()), "{s2}"((unsigned)blockIdx.x + (unsigned)(size_t)lds)
    : "v1", "v2", "v3", "v4", "v5", "v6", "v7", "v8", "v9", "v10", "v11", "v12", "v13", "v14", "v15", "v16", "v17", "v18", "v19", "v20", "v21", "v22", "v23", "v24", "v25", "v26", "v27", "v28", "v29", "v30", "v31", "v32", "v33", "v34", "v35", "v36", "v37", "v38", "v39", "v40", "v41", "v42", "v43", "v44", "v45", "v46", "v47", "v48", "v49", "v50", "v51", "v52", "v53", "v54", "v55", "v56", "v57", "v58", "v59", "v60", "v61", "v62", "v63", "v64", "v65", "v66", "v67", "v68", "v69", "v70", "v71", "v72", "v73", "v74", "v75", "v76", "v77", "v78", "v79", "v80", "v81", "v82", "v83", "v84", "v85", "v86", "v87", "v88", "v89", "v90", "v91", "v92", "v93", "v94", "v95", "v96", "v97", "v98", "v99", "v100", "v101", "v102", "v103", "v104", "v105", "v106", "v107", "v108", "v109", "v110", "v111", "v112", "v113", "v114", "v115", "v116", "v117", "v118", "v119", "v120", "v121", "v122", "v123", "v124", "v125", "v126", "v127", "v128", "v129", "v130", "v131", "v132", "v133", "v134", "v135", "v136", "v137", "v138", "v139", "v140", "v141", "v142", "v143", "v144", "v145", "v146", "v147", "v148", "v149", "v150", "v151", "v152", "v153", "v154", "v155", "v156", "v157", "v158", "v159", "v160", "v161", "v162", "v163", "v164", "v165", "v166", "v167", "v168", "v169", "v170", "v171", "v172", "v173", "v174", "v175", "v176", "v177", "v178", "v179", "v180", "v181", "v182", "v183", "v184", "v185", "v186", "v187", "v188", "v189", "v190", "v191", "v192", "v193", "v194", "v195", "v196", "v197", "v198", "v199", "v200", "v201", "v202", "v203", "v204", "v205", "v206", "v207", "v208", "v209", "v210", "v211", "v212", "v213", "v214", "v215", "v216", "v217", "v218", "v219", "v220", "v221", "v222", "v223", "v224", "v225", "v226", "v227", "v228", "v229", "v230", "v231", "v232", "v233", "v234", "v235", "v236", "v237", "v238", "v239", "v240", "v241", "v242", "v243", "v244", "v245", "v246", "v247", "v248", "v249", "v250", "v251", "v252", "v253", "v254", "v255", "a0", "a1", "a2", "a3", "a4", "a5", "a6", "a7", "a8", "a9", "a10", "a11", "a12", "a13", "a14", "a15", "a16", "a17", "a18", "a19", "a20", "a21", "a22", "a23", "a24", "a25", "a26", "a27", "a28", "a29", "a30", "a31", "a32", "a33", "a34", "a35", "a36", "a37", "a38", "a39", "a40", "a41", "a42", "a43", "a44", "a45", "a46", "a47", "a48", "a49", "a50", "a51", "a52", "a53", "a54", "a55", "a56", "a57", "a58", "a59", "a60", "a61", "a62", "a63", "a64", "a65", "a66", "a67", "a68", "a69", "a70", "a71", "a72", "a73", "a74", "a75", "a76", "a77", "a78", "a79", "a80", "a81", "a82", "a83", "a84", "a85", "a86", "a87", "a88", "a89", "a90", "a91", "a92", "a93", "a94", "a95", "a96", "a97", "a98", "a99", "a100", "a101", "a102", "a103", "a104", "a105", "a106", "a107", "a108", "a109", "a110", "a111", "a112", "a113", "a114", "a115", "a116", "a117", "a118", "a119", "a120", "a121", "a122", "a123", "a124", "a125", "a126", "a127", "a128", "a129", "a130", "a131", "a132", "a133", "a134", "a135", "a136", "a137", "a138", "a139", "a140", "a141", "a142", "a143", "a144", "a145", "a146", "a147", "a148", "a149", "a150", "a151", "a152", "a153", "a154", "a155", "a156", "a157", "a158", "a159", "a160", "a161", "a162", "a163", "a164", "a165", "a166", "a167", "a168", "a169", "a170", "a171", "a172", "a173", "a174", "a175", "a176", "a177", "a178", "a179", "a180", "a181", "a182", "a183", "a184", "a185", "a186", "a187", "a188", "a189", "a190", "a191", "a192", "a193", "a194", "a195", "a196", "a197", "a198", "a199", "a200", "a201", "a202", "a203", "a204", "a205", "a206", "a207", "a208", "a209", "a210", "a211", "a212", "a213", "a214", "a215", "a216", "a217", "a218", "a219", "a220", "a221", "a222", "a223", "a224", "a225", "a226", "a227", "a228", "a229", "a230", "a231", "a232", "a233", "a234", "a235", "a236", "a237", "a238", "a239", "a240", "a241", "a242", "a243", "a244", "a245", "a246", "a247", "a248", "a249", "a250", "a251", "a252", "a253", "a254", "a255", "s3", "s4", "s5", "s6", "s7", "s8", "s9", "s10", "s11", "s12", "s13", "s14", "s15", "s16", "s17", "s18", "s19", "s20", "s21", "s22", "s23", "s24", "s25", "s26", "s27", "s28", "s29", "s30", "s31", "s32", "s33", "s34", "s35", "s36", "s37", "s38", "s39", "s40", "s41", "s42", "s43", "s44", "s45", "s46", "s47", "s48", "s49", "s50", "s51", "s52", "s53", "s54", "s55", "s56", "s57", "s58", "s59", "s60", "s61", "s62", "s63", "s64", "s65", "s66", "s67", "s68", "s69", "s70", "s71", "s72", "s73", "s74", "s75", "s76", "s77", "s78", "s79", "s80", "s81", "s82", "s83", "s84", "s85", "s86", "s87", "s88", "s89", "s90", "s91", "s92", "s93", "s94", "s95", "s96", "s97", "s98", "s99", "vcc", "scc", "memory");
}

extern "C" void kernel_launch(void* const* d_in, const int* in_sizes, int n_in, void* d_out, int out_size, void* d_ws, size_t ws_size,
                              hipStream_t stream) {
  Params p;
  p.x = d_in[0]; p.kA = d_in[1]; p.vA = d_in[2]; p.wqA = d_in[3]; p.woA = d_in[4];
  p.kB = d_in[6]; p.vB = d_in[7]; p.wqB = d_in[8]; p.woB = d_in[9]; p.out = d_out; p.ws = d_ws;
  for (int i = 0; i < 8; ++i) p.tune[i] = g_tune[i];
  hipLaunchKernelGGL(mega_decode, dim3(256), dim3(256), 0, stream, p);
}
```
